```python
import torch

def _softmax(x, axis=-1):
    return torch.softmax(x, dim=axis)
def _gelu(x):
    return torch.nn.functional.gelu(x, approximate="tanh")
def _silu(x):
    return torch.nn.functional.silu(x)
def _sigmoid(x):
    return torch.sigmoid(x)
def _layernorm(x, axis=-1, eps=1e-5):
    m = x.mean(dim=axis, keepdim=True)
    v = x.var(dim=axis, keepdim=True, unbiased=False)
    return (x - m) / torch.sqrt(v + eps)
def _scatter_add(tgt, idx, src, axis=0):
    return tgt.clone().index_add_(axis, idx, src)
def _segment_sum(src, seg, n):
    out = torch.zeros((n,) + src.shape[1:], dtype=src.dtype)
    return out.index_add_(0, seg, src)
def _pad(x, pw):
    flat = tuple(v for pair in reversed(pw) for v in pair)
    return torch.nn.functional.pad(x, flat)

kernel_name = 'concat_heads__ssm_conv_s4d__linear_kernel_hedgehog__prompt'

def setup_inputs():

    _w = (lambda a: (lambda t: t.to(torch.bfloat16) if t.is_floating_point() else t)(torch.as_tensor(a)))
    def _A_setup():
        Bsz, L, D, N = 1, 2048, 576, 128
        return {
            "u": _w((lambda *s: torch.randn(*s, dtype=torch.bfloat16) * 0.05)(Bsz, L, D)),
            "log_A_real": _w((lambda *s: torch.randn(*s, dtype=torch.bfloat16) * 3.2)(D, N) * 0.5),
            "A_imag": _w((lambda *s: torch.randn(*s, dtype=torch.bfloat16) * 0.2)(D, N) * 3.14),
            "C_real": _w((lambda *s: torch.randn(*s, dtype=torch.bfloat16) * 3.2)(D, N) / N ** 0.5),
            "C_imag": _w((lambda *s: torch.randn(*s, dtype=torch.bfloat16) * 3.2)(D, N) / N ** 0.5),
            "log_dt": _w((lambda *s: torch.randn(*s, dtype=torch.bfloat16) * 0.2)(D) * 0.5 - 4.0),
            "D_skip": _w((lambda *s: torch.randn(*s, dtype=torch.bfloat16) * 0.4)(D)),
        }
    _A_in = _A_setup()

    def _B_setup():
        B, S, D, H = 1, 2048, 576, 32
        Dh = D // H
        return {
            "x": _w((lambda *s: torch.randn(*s, dtype=torch.bfloat16) * 0.4)(B, S, D)),
            "wqkv": _w((lambda *s: torch.randn(*s, dtype=torch.bfloat16) * 0.4)(D, 3 * D)),
            "w_phi": _w((lambda *s: torch.randn(*s, dtype=torch.bfloat16) * 0.4)(H, Dh, Dh) * (Dh ** -0.5)),
            "wo": _w((lambda *s: torch.randn(*s, dtype=torch.bfloat16) * 0.05)(D, D)),
        }
    _B_in = _B_setup()

    return {
        "A_u": _A_in["u"],
        "A_log_A_real": _A_in["log_A_real"],
        "A_A_imag": _A_in["A_imag"],
        "A_C_real": _A_in["C_real"],
        "A_C_imag": _A_in["C_imag"],
        "A_log_dt": _A_in["log_dt"],
        "A_D_skip": _A_in["D_skip"],
        "B_x": _B_in["x"],
        "B_wqkv": _B_in["wqkv"],
        "B_w_phi": _B_in["w_phi"],
        "B_wo": _B_in["wo"],
    }

def reference(A_u, A_log_A_real, A_A_imag, A_C_real, A_C_imag, A_log_dt, A_D_skip, B_x, B_wqkv, B_w_phi, B_wo):
    def _A_ref(u, log_A_real, A_imag, C_real, C_imag, log_dt, D_skip):
        L = u.shape[1]
        dt = torch.exp(log_dt)
        A = -torch.exp(log_A_real) + 1j * A_imag
        C = (C_real + 1j * C_imag) * dt[:, None]
        pos = (lambda x, dt: x.to(dt))(torch.arange(L), log_dt.dtype)
        P = torch.exp((A * dt[:, None])[:, :, None] * pos[None, None, :])
        K = 2.0 * torch.einsum("dn,dnl->dl", C, P).real
        L2 = 2 * L
        u_t = (lambda x, *d: x.permute(*d))(u, 0, 2, 1)
        Kf = (lambda x, *a, **k: torch.fft.rfft(x.float(), *a, **k))(K, L2)
        Uf = (lambda x, *a, **k: torch.fft.rfft(x.float(), *a, **k))(u_t, L2)
        y = (lambda x, *a, **k: torch.fft.irfft(x, *a, **k).to(torch.bfloat16))(Uf * Kf[None, :, :], L2)[:, :, :L]
        y = (lambda x, *d: x.permute(*d))(y, 0, 2, 1)
        return y + u * D_skip[None, None, :]

    def _B_ref(x, wqkv, w_phi, wo):
        B, S, D = x.shape
        H = 32; Dh = D // H
        qkv = (lambda x, *d: x.permute(*d))((x @ wqkv).reshape(B, S, 3, H, Dh), 2, 0, 3, 1, 4)
        q, k, v = qkv[0], qkv[1], qkv[2]
        pq = torch.einsum("bhsd,hdf->bhsf", q, w_phi)
        pk = torch.einsum("bhsd,hdf->bhsf", k, w_phi)
        phi_q = torch.concatenate([_softmax(pq, axis=-1), _softmax(-pq, axis=-1)], axis=-1)
        phi_k = torch.concatenate([_softmax(pk, axis=-1), _softmax(-pk, axis=-1)], axis=-1)
        kv = torch.einsum("bhsf,bhsd->bhfd", phi_k, v)
        z  = torch.einsum("bhsf->bhf", phi_k)
        num = torch.einsum("bhqf,bhfd->bhqd", phi_q, kv)
        den = torch.einsum("bhqf,bhf->bhq", phi_q, z)[..., None] + 1e-6
        o = num / den
        return (lambda x, *d: x.permute(*d))(o, 0, 2, 1, 3).reshape(B, S, D) @ wo

    a = _A_ref(A_u, A_log_A_real, A_A_imag, A_C_real, A_C_imag, A_log_dt, A_D_skip)
    b = _B_ref(B_x, B_wqkv, B_w_phi, B_wo)
    return torch.concatenate([a, b], axis=-1)
```

```cpp
#include <hip/hip_runtime.h>
#include <hip/hip_bf16.h>
typedef unsigned short u16;
__device__ __forceinline__ float b2f(u16 v){ return __uint_as_float(((unsigned)v)<<16); }
__device__ __forceinline__ u16 f2b(float f){ unsigned u=__float_as_uint(f); u += 0x7fffu + ((u>>16)&1u); return (u16)(u>>16); }
__device__ __forceinline__ float rb(float f){ return b2f(f2b(f)); }
constexpr int L=2048, D=576, N=128, H=32, DH=18;

__global__ void k_gen(const u16* lar, const u16* aim, const u16* cr, const u16* ci, const u16* ldt, float* K){
  int d = blockIdx.x;
  __shared__ float sa[N], sw[N], scr[N], sci[N];
  float dt = rb(expf(b2f(ldt[d])));
  if (threadIdx.x < N){ int n=threadIdx.x; sa[n] = -rb(expf(b2f(lar[d*N+n])))*dt; sw[n]=b2f(aim[d*N+n])*dt; scr[n]=b2f(cr[d*N+n])*dt; sci[n]=b2f(ci[d*N+n])*dt; }
  __syncthreads();
  for (int l=threadIdx.x; l<L; l+=blockDim.x){
    float p = rb((float)l); float acc=0;
    for (int n=0;n<N;n++){ float e=expf(sa[n]*p); float s,c; sincosf(sw[n]*p,&s,&c); acc += e*(scr[n]*c - sci[n]*s); }
    K[d*L+l]=2.f*acc;
  }
}
__global__ void k_conv(const u16* u, const float* K, const u16* dsk, u16* out){
  int d = blockIdx.x;
  __shared__ float su[L], sk[L];
  for (int l=threadIdx.x;l<L;l+=blockDim.x){ su[l]=b2f(u[l*D+d]); sk[l]=K[d*L+l]; }
  __syncthreads();
  float ds=b2f(dsk[d]);
  for (int l=threadIdx.x;l<L;l+=blockDim.x){ float acc=0; for(int j=0;j<=l;j++) acc+=sk[j]*su[l-j]; out[l*1152+d]=f2b(rb(acc)+rb(su[l]*ds)); }
}
__global__ void k_qkv(const u16* x, const u16* w, float* qkv){
  int s=blockIdx.x; __shared__ float sx[D];
  for(int i=threadIdx.x;i<D;i+=blockDim.x) sx[i]=b2f(x[s*D+i]);
  __syncthreads();
  for(int c=threadIdx.x;c<3*D;c+=blockDim.x){ float a=0; for(int k=0;k<D;k++) a+=sx[k]*b2f(w[k*3*D+c]); qkv[s*3*D+c]=rb(a);} }
__global__ void k_phi(const float* qkv, const u16* wphi, float* phi){
  int s=blockIdx.x; int t=threadIdx.x; if(t>=64) return; int which=t>>5, h=t&31;
  const float* q=qkv+s*3*D+which*D+h*DH; float p[DH]; float mx=-1e30f, mn=1e30f;
  for(int f=0;f<DH;f++){ float a=0; for(int dd=0;dd<DH;dd++) a+=q[dd]*b2f(wphi[h*DH*DH+dd*DH+f]); p[f]=rb(a); mx=fmaxf(mx,p[f]); mn=fminf(mn,p[f]); }
  float s1=0,s2=0; for(int f=0;f<DH;f++){ s1+=expf(p[f]-mx); s2+=expf(mn-p[f]); }
  float* o=phi+((size_t)(which*L+s)*H+h)*36;
  for(int f=0;f<DH;f++){ o[f]=rb(expf(p[f]-mx)/s1); o[18+f]=rb(expf(mn-p[f])/s2);} }
__global__ void k_kv(const float* qkv, const float* phi, float* kv){
  int h=blockIdx.x; for(int i=threadIdx.x;i<36*19;i+=blockDim.x){ int f=i/19,dd=i%19; float a=0; for(int s=0;s<L;s++){ float pk=phi[((size_t)(L+s)*H+h)*36+f]; a+= pk*(dd<18? qkv[s*3*D+2*D+h*DH+dd]:1.f);} kv[h*684+i]=rb(a);} }
__global__ void k_out(const float* phi, const float* kv, const u16* wo, u16* out){
  int s=blockIdx.x; __shared__ float so[D];
  for(int i=threadIdx.x;i<D;i+=blockDim.x){ int h=i/DH, dd=i%DH; const float* pq=phi+((size_t)s*H+h)*36; float nu=0,de=0; for(int f=0;f<36;f++){ nu+=pq[f]*kv[h*684+f*19+dd]; de+=pq[f]*kv[h*684+f*19+18]; } so[i]=rb(rb(nu)/rb(rb(de)+1e-6f)); }
  __syncthreads();
  for(int c=threadIdx.x;c<D;c+=blockDim.x){ float a=0; for(int k=0;k<D;k++) a+=so[k]*b2f(wo[k*D+c]); out[s*1152+576+c]=f2b(a);} }

extern "C" void kernel_launch(void* const* d_in, const int* in_sizes, int n_in, void* d_out, int out_size, void* d_ws, size_t ws_size, hipStream_t stream){
  const u16* u=(const u16*)d_in[0]; u16* out=(u16*)d_out;
  float* K=(float*)d_ws; float* qkv=K+D*L; float* phi=qkv+L*3*D; float* kv=phi+2*L*H*36;
  k_gen<<<D,256,0,stream>>>((const u16*)d_in[1],(const u16*)d_in[2],(const u16*)d_in[3],(const u16*)d_in[4],(const u16*)d_in[5],K);
  k_conv<<<D,256,0,stream>>>(u,K,(const u16*)d_in[6],out);
  k_qkv<<<L,256,0,stream>>>((const u16*)d_in[7],(const u16*)d_in[8],qkv);
  k_phi<<<L,64,0,stream>>>(qkv,(const u16*)d_in[9],phi);
  k_kv<<<H,256,0,stream>>>(qkv,phi,kv);
  k_out<<<L,256,0,stream>>>(phi,kv,(const u16*)d_in[10],out);
}
```

```cpp
#include <hip/hip_runtime.h>
#include <type_traits>

typedef unsigned short u16;
typedef unsigned long long u64;
typedef unsigned v4u __attribute__((ext_vector_type(4)));
typedef unsigned v3u __attribute__((ext_vector_type(3)));
typedef unsigned v2u __attribute__((ext_vector_type(2)));
typedef float f32x4 __attribute__((ext_vector_type(4)));
typedef float f32x2 __attribute__((ext_vector_type(2)));
typedef short s16x4 __attribute__((ext_vector_type(4)));
typedef __bf16 bf16x8 __attribute__((ext_vector_type(8)));
typedef __bf16 bf16x2 __attribute__((ext_vector_type(2)));
typedef _Float16 f16x8 __attribute__((ext_vector_type(8)));
typedef _Float16 f16x2 __attribute__((ext_vector_type(2)));
#define LAS __attribute__((address_space(3)))
#define GAS __attribute__((address_space(1)))
#define DI __device__ __forceinline__
typedef LAS char* lptr;

constexpr int WS_EPOCH = 0;
constexpr int WS_F0 = 256;
constexpr int WS_F1 = 4096;
constexpr int WS_F2 = 8192;
constexpr int WS_PART = 16384;
constexpr int PART_B = 5632;
constexpr int WS_OM = 1 << 20;
constexpr int WS_OS = 3 << 20;
constexpr int WS_DBG = 4 << 20;
constexpr int WS_USED = 5 << 20;
constexpr int LDS_BYTES = 160 * 1024;

constexpr int LB_W = 0, LB_WSLOT = 24576, LB_X = 73728, LB_WPHI = 141312;
constexpr int LB_IMG = 0, L_WO = 57344, LB_KVF = 143360;
constexpr int L_RED = 0, L_OST = 20480;
constexpr int LA_STG = 34272;
constexpr int LA_KC = 4160, LA_UC = 4640, LA_U0 = 4 * LA_KC, LA_CH = 4 * LA_KC + 2 * LA_UC;
constexpr int LA_RED = 103680;
constexpr int LA_OUT = 140544;

#define PROFW 0
#define STAMP(i) do {} while (0)

DI float bf2f(unsigned lo16) { return __uint_as_float(lo16 << 16); }
DI unsigned pk_bf16(float a, float b) { f32x2 v = {a, b}; return __builtin_bit_cast(unsigned, __builtin_convertvector(v, bf16x2)); }
DI unsigned pk_f16(float a, float b) { f32x2 v = {a, b}; return __builtin_bit_cast(unsigned, __builtin_convertvector(v, f16x2)); }
DI float rnd_bf16(float a) { return bf2f(pk_bf16(a, a) & 0xffffu); }
DI f32x4 mfma_bf(v4u a, v4u b, f32x4 c) { return __builtin_amdgcn_mfma_f32_16x16x32_bf16(__builtin_bit_cast(bf16x8, a), __builtin_bit_cast(bf16x8, b), c, 0, 0, 0); }
DI f32x4 mfma_h(v4u a, v4u b, f32x4 c) { return __builtin_amdgcn_mfma_f32_16x16x32_f16(__builtin_bit_cast(f16x8, a), __builtin_bit_cast(f16x8, b), c, 0, 0, 0); }
DI v2u trd(lptr p) { return __builtin_bit_cast(v2u, __builtin_amdgcn_ds_read_tr16_b64_v4i16((LAS s16x4*)p)); }
DI v4u trd2(lptr p0, lptr p1) { v2u a = trd(p0), b = trd(p1); return (v4u){a.x, a.y, b.x, b.y}; }
DI v4u lds16(lptr p) { return *(LAS v4u*)p; }
template <int IMM> DI void glds(const char* base, unsigned voff, unsigned lbase) {
  asm volatile("s_add_u32 m0, %2, %3\n\ts_nop 0\n\tglobal_load_lds_dwordx4 %0, %1" :: "v"(voff), "s"(base), "s"(lbase), "i"(IMM) : "memory", "scc");
}
#define IC(n) std::integral_constant<int, (n)>{}
template <int N, int I = 0, class F> DI void static_for(F&& f) { if constexpr (I < N) { f(std::integral_constant<int, I>{}); static_for<N, I + 1>(f); } }
DI void gld32(unsigned& d, const char* base, unsigned voff) { asm volatile("global_load_dword %0, %1, %2" : "=v"(d) : "v"(voff), "s"(base) : "memory"); }
DI void gld16(unsigned& d, const char* base, unsigned voff) { asm volatile("global_load_ushort %0, %1, %2" : "=v"(d) : "v"(voff), "s"(base) : "memory"); }
DI void gld96(v3u& d, const char* base, unsigned voff) { asm volatile("global_load_dwordx3 %0, %1, %2" : "=v"(d) : "v"(voff), "s"(base) : "memory"); }
#define WAITCNT(vm, lgkm) __builtin_amdgcn_s_waitcnt(((vm) & 15) | (((vm) >> 4) << 14) | 0x70 | ((lgkm) << 8))
#define BAR() __builtin_amdgcn_s_barrier()
#define SCHED() __builtin_amdgcn_sched_barrier(0)
#define DRAIN() asm volatile("s_waitcnt vmcnt(0)" ::: "memory")
DI void lds_barrier() { asm volatile("" ::: "memory"); WAITCNT(63, 0); BAR(); asm volatile("" ::: "memory"); }
DI f32x4 sel4(bool c, f32x4 a, f32x4 b) { return (f32x4){c ? a[0] : b[0], c ? a[1] : b[1], c ? a[2] : b[2], c ? a[3] : b[3]}; }
DI void ds_write2(unsigned addr, unsigned a, unsigned b) { asm volatile("ds_write2_b32 %0, %1, %2 offset1:1" :: "v"(addr), "v"(a), "v"(b) : "memory"); }

DI u64 mix64(u64 z) { z += 0x9E3779B97F4A7C15ull; z = (z ^ (z >> 30)) * 0xBF58476D1CE4E5B9ull; z = (z ^ (z >> 27)) * 0x94D049BB133111EBull; return z ^ (z >> 31); }
DI u64 read_tag(const char* ws) {
  u64 e; asm volatile("s_load_dwordx2 %0, %1, 0x0\n\ts_waitcnt lgkmcnt(0)" : "=s"(e) : "s"(ws) : "memory");
  return mix64(e);
}
DI void flag_store(char* p, u64 tag) { __hip_atomic_store((GAS u64*)p, tag, __ATOMIC_RELAXED, __HIP_MEMORY_SCOPE_AGENT); }
DI void poll_flags(char* p, int n, u64 tag, int lane) {
  GAS u64* f = (GAS u64*)p + (lane < n ? lane : 0);
  for (unsigned spins = 0; spins < (1u << 22); ++spins) {
    u64 v = __hip_atomic_load(f, __ATOMIC_RELAXED, __HIP_MEMORY_SCOPE_AGENT);
    if (__all(v == tag)) break;
    __builtin_amdgcn_s_sleep(1);
  }
  __builtin_amdgcn_fence(__ATOMIC_ACQUIRE, "wavefront");
}

DI void swap32(float& a, float& b) { auto r = __builtin_amdgcn_permlane32_swap(__float_as_uint(a), __float_as_uint(b), false, false); a = __uint_as_float(r[0]); b = __uint_as_float(r[1]); }
DI void swap16(float& a, float& b) { auto r = __builtin_amdgcn_permlane16_swap(__float_as_uint(a), __float_as_uint(b), false, false); a = __uint_as_float(r[0]); b = __uint_as_float(r[1]); }

DI void features(const f32x4& F0, const f32x4& F1, float e, v4u& P0, v4u& P1, unsigned& PX) {
  const float L = 1.4426950408889634f;
  float a = fmaxf(fmaxf(F0[0], F0[1]), fmaxf(F0[2], F0[3])), b = fmaxf(fmaxf(F1[0], F1[1]), fmaxf(F1[2], F1[3]));
  float c = -fminf(fminf(F0[0], F0[1]), fminf(F0[2], F0[3])), d = -fminf(fminf(F1[0], F1[1]), fminf(F1[2], F1[3]));
  swap32(a, b); swap32(c, d);
  float r1 = fmaxf(fmaxf(a, b), e), r2 = fmaxf(fmaxf(c, d), -e);
  swap16(r1, r2);
  float t1 = fmaxf(r1, r2) * -L, t2 = t1;
  swap16(t1, t2);
  float m0 = t1, m1 = t1, n0 = t2, n1 = t2;
  swap32(m0, m1); swap32(n0, n1);
  float p0[4], q0[4], p1[4], q1[4];
#pragma unroll
  for (int j = 0; j < 4; ++j) {
    p0[j] = __builtin_amdgcn_exp2f(fmaf(F0[j], L, m0)); q0[j] = __builtin_amdgcn_exp2f(fmaf(F0[j], -L, n0));
    p1[j] = __builtin_amdgcn_exp2f(fmaf(F1[j], L, m1)); q1[j] = __builtin_amdgcn_exp2f(fmaf(F1[j], -L, n1));
  }
  float px = __builtin_amdgcn_exp2f(fmaf(e, L, t1)), qx = __builtin_amdgcn_exp2f(fmaf(e, -L, t2));
  a = (p0[0] + p0[1]) + (p0[2] + p0[3]); b = (p1[0] + p1[1]) + (p1[2] + p1[3]);
  c = (q0[0] + q0[1]) + (q0[2] + q0[3]); d = (q1[0] + q1[1]) + (q1[2] + q1[3]);
  swap32(a, b); swap32(c, d);
  r1 = a + b + px; r2 = c + d + qx;
  swap16(r1, r2);
  t1 = __builtin_amdgcn_rcpf(r1 + r2); t2 = t1;
  swap16(t1, t2);
  m0 = t1; m1 = t1; n0 = t2; n1 = t2;
  swap32(m0, m1); swap32(n0, n1);
  P0 = (v4u){pk_bf16(p0[0] * m0, p0[1] * m0), pk_bf16(p0[2] * m0, p0[3] * m0), pk_bf16(q0[0] * n0, q0[1] * n0), pk_bf16(q0[2] * n0, q0[3] * n0)};
  P1 = (v4u){pk_bf16(p1[0] * m1, p1[1] * m1), pk_bf16(p1[2] * m1, p1[3] * m1), pk_bf16(q1[0] * n1, q1[1] * n1), pk_bf16(q1[2] * n1, q1[3] * n1)};
  PX = pk_bf16(px * t1, qx * t2);
}

struct cplx2 { f32x2 r, i; };
DI cplx2 cmul(const cplx2& a, const cplx2& b) { cplx2 c; c.r = a.r * b.r - a.i * b.i; c.i = a.r * b.i + a.i * b.r; return c; }
DI cplx2 cexp2(f32x2 al, f32x2 wr, float p) {
  cplx2 c;
#pragma unroll
  for (int k = 0; k < 2; ++k) {
    float m = __builtin_amdgcn_exp2f(al[k] * p), f = __builtin_amdgcn_fractf(wr[k] * p);
    c.r[k] = m * __builtin_amdgcn_cosf(f); c.i[k] = m * __builtin_amdgcn_sinf(f);
  }
  return c;
}
DI void put_row(lptr p, const cplx2& v) { *(LAS v2u*)p = (v2u){pk_f16(v.r[0], v.i[0]), pk_f16(v.r[1], v.i[1])}; }

DI void stage_wo(const char* wo, int cg, unsigned lw1k, int w, int lane) {
  int ln = lane; asm volatile("" : "+v"(ln));
  const char* b = wo + cg * 144;
  static_for<11>([&](auto I) {
    constexpr int i = I;
    if (i < 10 || w == 0) {
      const unsigned c = (i * 8 + w) * 64 + ln, r = (c * 7282u) >> 16;
      glds<L_WO + i * 8192>(b, r * 1008u + c * 16u, lw1k);
    }
  });
}

__global__ __launch_bounds__(512, 2) void fused_kernel(
    const char* __restrict__ g_u, const char* __restrict__ g_lar, const char* __restrict__ g_aim, const char* __restrict__ g_cr,
    const char* __restrict__ g_ci, const char* __restrict__ g_ldt, const char* __restrict__ g_dsk, const char* __restrict__ g_x,
    const char* __restrict__ g_w, const char* __restrict__ g_wphi, const char* __restrict__ g_wo, char* __restrict__ g_out, char* g_ws) {
  extern __shared__ __attribute__((aligned(16))) char smem[];
  const lptr lds = (lptr)smem;
  const int tid = threadIdx.x, lane = tid & 63, w = __builtin_amdgcn_readfirstlane(tid >> 6) & 7;
  const int blk = blockIdx.x, xcd = blk & 7, bi = blk >> 3;
  const int g = lane >> 4, s16 = lane & 15, tq = (lane >> 2) & 3, tp = lane & 3;
  const int cg = bi & 7;
  const unsigned lw1k = (unsigned)(size_t)lds + w * 1024;
  STAMP(0);
  asm volatile("" :: "s"(g_u), "s"(g_lar), "s"(g_aim), "s"(g_cr), "s"(g_ci), "s"(g_ldt), "s"(g_dsk), "s"(g_x), "s"(g_w), "s"(g_wphi), "s"(g_wo), "s"(g_out), "s"(g_ws));
  const __amdgpu_buffer_rsrc_t ws_rs = __builtin_amdgcn_make_buffer_rsrc(g_ws, 0, WS_USED, 0x00020000);
  u64 tag;

  if (bi < 16) {
    const int rb = (xcd >> 1) * 2 + (bi >> 3), hp = (xcd & 1) * 8 + (bi & 7);
    const int lr = lane >> 3, lc = lane & 7;
    const int wrow = 8 * w + lr;
    unsigned w_off = wrow * 3456u + (((72 * hp) & ~63) + ((lc ^ ((wrow & 2) | ((wrow >> 1) & 4))) << 4));
    const char* xb0 = g_x + (size_t)(rb * 256 + w * 32) * 1152;
    const char* xb1 = xb0 + 16 * 1152;
    const char *wb1 = g_w + 1152, *wb2 = g_w + 2304;
    unsigned x_off0 = lr * 1152u + ((lc ^ (lane >> 4)) << 4), x_off1 = (8 + lr) * 1152u + ((lc ^ (4 + (lane >> 4))) << 4);
    const unsigned lw8k = (unsigned)(size_t)lds + w * 8192;
    auto issue_w = [&](auto T) {
      constexpr int o = LB_W + (T % 3) * LB_WSLOT;
      glds<o>(g_w, w_off, lw1k); glds<o + 8192>(wb1, w_off, lw1k); glds<o + 16384>(wb2, w_off, lw1k);
      w_off += 64 * 3456; asm volatile("" : "+v"(w_off));
    };
    auto issue_x = [&](auto T) {
      constexpr int o = LB_X + (T & 1) * 4096;
      glds<o>(xb0, x_off0, lw8k); glds<o + 1024>(xb0, x_off1, lw8k); glds<o + 2048>(xb1, x_off0, lw8k); glds<o + 3072>(xb1, x_off1, lw8k);
      x_off0 += 128; x_off1 += 128; asm volatile("" : "+v"(x_off0), "+v"(x_off1));
    };
    issue_w(IC(0)); issue_x(IC(0));
    if (w < 2) { const int c = w * 64 + lane; glds<LB_WPHI>(g_wphi + hp * 1296, (c < 81 ? c : 80) * 16, lw1k); }
    issue_w(IC(1)); issue_x(IC(1)); issue_w(IC(2));
    tag = read_tag(g_ws);
    const int ub = ((72 * hp) & 63) >> 3, fsw = (tq & 2) | ((g & 1) << 2), trow = (8 * g + tq) * 128;
    const lptr wa0 = lds + trow + ((((ub + tp) >> 1) ^ fsw) << 4) + ((ub + tp) & 1) * 8;
    const lptr wa1 = lds + trow + ((((ub + 5 + tp) >> 1) ^ fsw) << 4) + ((ub + 5 + tp) & 1) * 8;
    const lptr was = lds + trow + ((((ub + 4) >> 1) ^ fsw) << 4) + ((ub + 4) & 1) * 8 + (tp < 2 ? tp : 2) * 8192;
    const lptr xa0 = lds + LB_X + w * 8192 + s16 * 128 + ((g ^ (s16 >> 1)) << 4);
    const lptr xa1 = lds + LB_X + w * 8192 + s16 * 128 + (((4 + g) ^ (s16 >> 1)) << 4);
    auto read_w = [&](v4u (&f)[7], int t, int ks) {
      const int o = (t % 3) * LB_WSLOT + ks * 4096;
#pragma unroll
      for (int p = 0; p < 3; ++p) {
        f[2 * p] = trd2(wa0 + o + p * 8192, wa0 + o + p * 8192 + 512);
        f[2 * p + 1] = trd2(wa1 + o + p * 8192, wa1 + o + p * 8192 + 512);
      }
      f[6] = trd2(was + o, was + o + 512);
    };
    auto read_x = [&](v4u (&f)[4], int t) {
      const int o = (t & 1) * 4096;
      f[0] = lds16(xa0 + o); f[1] = lds16(xa0 + o + 2048); f[2] = lds16(xa1 + o); f[3] = lds16(xa1 + o + 2048);
    };
    f32x4 acc[7][2];
#pragma unroll
    for (int n = 0; n < 7; ++n) { acc[n][0] = (f32x4){0.f, 0.f, 0.f, 0.f}; acc[n][1] = acc[n][0]; }
    v4u wf0[7], wf1[7], xc[4], xn[4];
    unsigned wm[4][2], wsp[4];
    WAITCNT(10, 15); BAR(); asm volatile("" ::: "memory");
    STAMP(1);
    if (tid == 0) flag_store(g_ws + WS_F0 + blk * 8, tag);
    read_x(xc, 0); read_w(wf0, 0, 0);
    WAITCNT(63, 0); asm volatile("" ::: "memory");
    issue_x(IC(2));
    static_for<9>([&](auto T) {
      constexpr int t = T;
      SCHED();
      read_w(wf1, t, 1);
      if (t == 2) {
        const lptr wp = lds + LB_WPHI;
#pragma unroll
        for (int fi = 0; fi < 4; ++fi) {
          const int hh = fi & 1, f = fi < 2 ? s16 : 16 + ((s16 >> 2) & 1);
          const lptr b = wp + hh * 648 + f * 2;
          const int d0 = 4 * g + 2 * hh, ds = hh ? 0 : 16;
          unsigned e0 = *(LAS u16*)(b + d0 * 36), e1 = *(LAS u16*)(b + d0 * 36 + 36), e2 = *(LAS u16*)(b + d0 * 36 + 72), e3 = *(LAS u16*)(b + d0 * 36 + 108);
          unsigned e4 = *(LAS u16*)(b + ds * 36), e5 = *(LAS u16*)(b + ds * 36 + 36);
          const bool z = fi >= 2 && (s16 >> 3) != hh;
          wm[fi][0] = z ? 0u : (e0 | (e1 << 16)); wm[fi][1] = z ? 0u : (e2 | (e3 << 16)); wsp[fi] = z ? 0u : (e4 | (e5 << 16));
        }
      }
      SCHED();
#pragma unroll
      for (int n = 0; n < 7; ++n) { acc[n][0] = mfma_bf(wf0[n], xc[0], acc[n][0]); acc[n][1] = mfma_bf(wf0[n], xc[1], acc[n][1]); }
      SCHED();
      if (t < 7) WAITCNT(7, 0); else WAITCNT(0, 0);
      BAR(); asm volatile("" ::: "memory");
      if constexpr (t < 6) issue_w(IC(t + 3));
      if (t == 8) stage_wo(g_wo, cg, lw1k, w, lane);
      SCHED();
      if (t < 8) { read_x(xn, t + 1); read_w(wf0, t + 1, 0); }
      SCHED();
#pragma unroll
      for (int n = 0; n < 7; ++n) { acc[n][0] = mfma_bf(wf1[n], xc[2], acc[n][0]); acc[n][1] = mfma_bf(wf1[n], xc[3], acc[n][1]); }
      SCHED();
      if (t < 8) {
        WAITCNT(63, 0); asm volatile("" ::: "memory");
        if constexpr (t < 6) issue_x(IC(t + 3));
#pragma unroll
        for (int i = 0; i < 4; ++i) xc[i] = xn[i];
      }
    });
    STAMP(2);
    auto phi = [&](int part, int m, v4u& P0, v4u& P1, unsigned& PX) {
      const f32x4 &T0 = acc[2 * part][m], &T1 = acc[2 * part + 1][m], &S = acc[6][m];
      const unsigned s0 = pk_bf16(S[0], S[1]), s1 = pk_bf16(S[2], S[3]);
      const v4u B0 = {pk_bf16(T0[0], T0[1]), pk_bf16(T0[2], T0[3]), s0, s1}, B1 = {pk_bf16(T1[0], T1[1]), pk_bf16(T1[2], T1[3]), s0, s1};
      const bool on = g == part;
      const f32x4 z = {0.f, 0.f, 0.f, 0.f};
      const f32x4 F0 = mfma_bf((v4u){wm[0][0], wm[0][1], on ? wsp[0] : 0u, 0u}, B0, z);
      const f32x4 F1 = mfma_bf((v4u){wm[1][0], wm[1][1], 0u, on ? wsp[1] : 0u}, B1, z);
      f32x4 F2 = mfma_bf((v4u){wm[2][0], wm[2][1], on ? wsp[2] : 0u, 0u}, B0, z);
      F2 = mfma_bf((v4u){wm[3][0], wm[3][1], 0u, on ? wsp[3] : 0u}, B1, F2);
      features(F0, F1, F2[0], P0, P1, PX);
    };
    {
      const int sw = s16 >> 2;
#pragma unroll
      for (int m = 0; m < 2; ++m) {
        v4u P0, P1; unsigned PX;
        phi(1, m, P0, P1, PX);
        const lptr row = lds + LB_IMG + (32 * w + 16 * m + s16) * 224, rp = row + ((g ^ sw) << 3);
        *(LAS v2u*)(rp) = (v2u){P0.x, P0.y}; *(LAS v2u*)(rp + 32) = (v2u){P0.z, P0.w};
        *(LAS v2u*)(rp + 64) = (v2u){P1.x, P1.y}; *(LAS v2u*)(rp + 96) = (v2u){P1.z, P1.w};
        *(LAS unsigned*)(row + 128 + (((g >> 1) ^ sw) << 3) + (g & 1) * 4) = PX;
        const f32x4 &V0 = acc[4][m], &V1 = acc[5][m], &S = acc[6][m];
        *(LAS v2u*)(rp + 160) = (v2u){pk_bf16(V0[0], V0[1]), pk_bf16(V0[2], V0[3])};
        *(LAS v2u*)(rp + 192) = (v2u){pk_bf16(V1[0], V1[1]), pk_bf16(V1[2], V1[3])};
        if (g >= 2) *(LAS v2u*)(rp + 128) = g == 2 ? (v2u){pk_bf16(S[0], S[1]), pk_bf16(S[2], S[3])} : (v2u){0x3f803f80u, 0x3f803f80u};
      }
    }
    lds_barrier();
    STAMP(3);
    {
      const int a = w & 3, h = a >> 1;
      const lptr ib = lds + LB_IMG + (4 * g + tq) * 224 + ((tp ^ g) << 3);
      const lptr pa = ib + a * 32, pb = ib + (w < 4 ? 160 + 32 * h : 128), pe = ib + 128;
      const bool ex = (w & 5) == 0 || w == 4;
      f32x4 c0 = {0.f, 0.f, 0.f, 0.f}, c1 = c0;
#pragma unroll
      for (int ks = 0; ks < 8; ++ks) {
        const int o = ks * 32 * 224;
        const v4u A = trd2(pa + o, pa + o + 16 * 224), B = trd2(pb + o, pb + o + 16 * 224);
        c0 = mfma_bf(A, B, c0);
        if (ex) c1 = mfma_bf(trd2(pe + o, pe + o + 16 * 224), B, c1);
      }
      const unsigned pbase = WS_PART + (hp * 8 + rb) * PART_B;
      if (w < 4) {
        __builtin_amdgcn_raw_buffer_store_b128(__builtin_bit_cast(v4u, c0), ws_rs, pbase + (a * 64 + lane) * 16, 0, 16);
        if (ex && g == h) __builtin_amdgcn_raw_buffer_store_b128(__builtin_bit_cast(v4u, c1), ws_rs, pbase + (304 + h * 16 + s16) * 16, 0, 16);
      } else {
        const int rec = s16 == 12 ? 0 : s16 - 7 - 2 * h;
        if (s16 == 12 || rec == 1 || rec == 2) __builtin_amdgcn_raw_buffer_store_b128(__builtin_bit_cast(v4u, c0), ws_rs, pbase + (256 + (a * 3 + rec) * 4 + g) * 16, 0, 16);
        const int rx = s16 == 12 ? 0 : s16 - 7 - 2 * g;
        if (ex && g < 2 && (s16 == 12 || rx == 1 || rx == 2)) __builtin_amdgcn_raw_buffer_store_b128(__builtin_bit_cast(v4u, c1), ws_rs, pbase + (336 + g * 3 + rx) * 16, 0, 16);
      }
    }
    DRAIN(); BAR(); asm volatile("" ::: "memory");
    if (tid == 0) flag_store(g_ws + WS_F1 + hp * 128 + rb * 8, tag);
    STAMP(4);
    v4u Q0[2], Q1[2]; unsigned QX[2];
    phi(0, 0, Q0[0], Q1[0], QX[0]); phi(0, 1, Q0[1], Q1[1], QX[1]);
    STAMP(5);
    if (w == 0) poll_flags(g_ws + WS_F1 + hp * 128, 8, tag, lane);
    BAR(); asm volatile("" ::: "memory");
    STAMP(6);
    if (tid < 342) {
      v4u pv[8];
#pragma unroll
      for (int r = 0; r < 8; ++r) pv[r] = __builtin_amdgcn_raw_buffer_load_b128(ws_rs, WS_PART + hp * 8 * PART_B + tid * 16, r * PART_B, 16);
      f32x4 sm = __builtin_bit_cast(f32x4, pv[0]);
#pragma unroll
      for (int r = 1; r < 8; ++r) sm += __builtin_bit_cast(f32x4, pv[r]);
      int dst;
      if (tid < 256) dst = (tid >> 7) * 1024 + (tid & 63) * 16 + ((tid >> 6) & 1) * 8;
      else if (tid < 304) { const int i = tid - 256, a = i / 12, rec = (i >> 2) - a * 3; dst = 2048 + (a >> 1) * 256 + rec * 64 + (i & 3) * 16 + (a & 1) * 8; }
      else if (tid < 336) dst = 2560 + (tid - 304) * 8;
      else { const int i = tid - 336; dst = 2816 + (i < 3 ? i : i + 1) * 8; }
      *(LAS v2u*)(lds + LB_KVF + dst) = (v2u){pk_bf16(sm[0], sm[1]), pk_bf16(sm[2], sm[3])};
    }
    lds_barrier();
    STAMP(7);
    {
      const lptr kb = lds + LB_KVF;
      const int rec = tp < 3 ? tp : 0;
      v4u Nm[2], Em[2]; unsigned Nx[2], Ex[2];
#pragma unroll
      for (int h = 0; h < 2; ++h) {
        Nm[h] = lds16(kb + h * 1024 + lane * 16); Em[h] = lds16(kb + 2048 + h * 256 + rec * 64 + g * 16);
        Nx[h] = *(LAS unsigned*)(kb + 2560 + h * 128 + s16 * 8 + (g & 1) * 4); Ex[h] = *(LAS unsigned*)(kb + 2816 + h * 32 + rec * 8 + (g & 1) * 4);
      }
      v2u sp[2];
#pragma unroll
      for (int m = 0; m < 2; ++m) {
        f32x4 O[2], E[2];
#pragma unroll
        for (int h = 0; h < 2; ++h) {
          const v4u Bm = h ? Q1[m] : Q0[m], Bx = {(g >> 1) == h ? QX[m] : 0u, 0u, 0u, 0u};
          const f32x4 z = {0.f, 0.f, 0.f, 0.f};
          O[h] = mfma_bf((v4u){Nx[h], 0u, 0u, 0u}, Bx, mfma_bf(Nm[h], Bm, z));
          E[h] = mfma_bf((v4u){Ex[h], 0u, 0u, 0u}, Bx, mfma_bf(Em[h], Bm, z));
        }
        const float r0 = __builtin_amdgcn_rcpf(E[0][0] + 1e-6f), r1 = __builtin_amdgcn_rcpf(E[1][0] + 1e-6f);
        const v4u ov = {pk_bf16(O[0][0] * r0, O[0][1] * r0), pk_bf16(O[0][2] * r0, O[0][3] * r0), pk_bf16(O[1][0] * r1, O[1][1] * r1), pk_bf16(O[1][2] * r1, O[1][3] * r1)};
        __builtin_amdgcn_raw_buffer_store_b128(ov, ws_rs, WS_OM + ((rb * 16 + 2 * w + m) * 16 + hp) * 1024 + lane * 16, 0, 16);
        sp[m] = (v2u){pk_bf16(E[0][1] * r0, E[0][2] * r0), pk_bf16(E[1][1] * r1, E[1][2] * r1)};
      }
      if (g < 2) __builtin_amdgcn_raw_buffer_store_b64((v2u){g ? sp[1].x : sp[0].x, g ? sp[1].y : sp[0].y}, ws_rs, WS_OS + ((rb * 16 + hp) * 256 + 32 * w + lane) * 8, 0, 16);
    }
    DRAIN(); BAR(); asm volatile("" ::: "memory");
    if (tid == 0) flag_store(g_ws + WS_F2 + rb * 128 + hp * 8, tag);
    STAMP(8);
  } else {
    const int ab = xcd * 16 + (bi - 16);
    const int c0 = 9 * (ab >> 1) + ((ab & 1) ? 5 : 0), nch = (ab & 1) ? 4 : 5;
    const int cs = w >> 1, hv = w & 1;
    unsigned prm[2][4]; unsigned pdt[2], pds[2];
#pragma unroll
    for (int r = 0; r < 2; ++r) {
      const int ch = r ? c0 + nch - 1 : c0 + cs;
      const unsigned o = ch * 256 + lane * 4;
      gld32(prm[r][0], g_lar, o); gld32(prm[r][1], g_aim, o); gld32(prm[r][2], g_cr, o); gld32(prm[r][3], g_ci, o);
      gld16(pdt[r], g_ldt, ch * 2); gld16(pds[r], g_dsk, ch * 2);
    }
    unsigned pf[2];
#pragma unroll
    for (int j = 0; j < 2; ++j) {
      unsigned i = j * 8192 + (bi - 16) * 512 + tid; if (i > 13247) i = 13247;
      const unsigned kt = i / 1472u, r = i - kt * 1472u;
      const unsigned r2 = r - 512u, row = r2 / 15u, q = r2 - row * 15u, part = q / 5u;
      const char* p = r < 512u ? g_x + ((xcd >> 1) * 512 + r) * 1152u + kt * 128u
                               : g_w + (kt * 64u + row) * 3456u + part * 1152u + ((xcd & 1) * 4 + q - part * 5u) * 128u;
      asm volatile("global_load_dword %0, %1, off" : "=v"(pf[j]) : "v"(p) : "memory");
    }
    int ua = (2 * c0) & ~3; if (ua > 1140) ua = 1140;
    const int ush = c0 - (ua >> 1);
    const int rt = (tid + (bi - 16) * 64) & 511;
    v3u ur[4], uh;
#pragma unroll
    for (int j = 0; j < 4; ++j) gld96(ur[j], g_u + ua, (rt * 4 + j) * 1152u);
    const int hr = (rt & ~63) * 4 - 1;
    gld96(uh, g_u + ua, (hr < 0 ? 0 : hr) * 1152u);
    tag = read_tag(g_ws);
    asm volatile("s_waitcnt vmcnt(7)" : "+v"(prm[0][0]), "+v"(prm[0][1]), "+v"(prm[0][2]), "+v"(prm[0][3]), "+v"(prm[1][0]), "+v"(prm[1][1]), "+v"(prm[1][2]),
                 "+v"(prm[1][3]), "+v"(pdt[0]), "+v"(pdt[1]), "+v"(pds[0]), "+v"(pds[1]) :: "memory");
    STAMP(6);
    if (tid == 0) flag_store(g_ws + WS_F0 + blk * 8, tag);

    auto round = [&](auto RD) {
      constexpr int rd = RD;
      const bool act = rd == 0 || w < 2;
      const int ns = rd == 0 ? 4 : 1;
      float kscale = 0.f, dskip = 0.f;
      if (act) {
        const unsigned q0 = rd ? prm[1][0] : prm[0][0], q1 = rd ? prm[1][1] : prm[0][1], q2 = rd ? prm[1][2] : prm[0][2], q3 = rd ? prm[1][3] : prm[0][3];
        const float dt = rnd_bf16(expf(bf2f(rd ? pdt[1] : pdt[0])));
        dskip = bf2f(rd ? pds[1] : pds[0]);
        f32x2 al, wr; cplx2 C;
        al[0] = -rnd_bf16(expf(bf2f(q0 & 0xffffu))) * dt * 1.4426950408889634f; al[1] = -rnd_bf16(expf(bf2f(q0 >> 16))) * dt * 1.4426950408889634f;
        wr[0] = bf2f(q1 & 0xffffu) * dt * 0.15915494309189535f; wr[1] = bf2f(q1 >> 16) * dt * 0.15915494309189535f;
        C.r[0] = bf2f(q2 & 0xffffu); C.r[1] = bf2f(q2 >> 16); C.i[0] = bf2f(q3 & 0xffffu); C.i[1] = bf2f(q3 >> 16);
        float mx = fmaxf(fmaxf(fabsf(C.r[0]), fabsf(C.r[1])), fmaxf(fabsf(C.i[0]), fabsf(C.i[1])));
#pragma unroll
        for (int o = 32; o >= 1; o >>= 1) mx = fmaxf(mx, __shfl_xor(mx, o));
        int ex = (int)((__float_as_uint(mx) >> 23) & 255u); ex = ex < 30 ? 30 : (ex > 230 ? 230 : ex);
        const float sc = __uint_as_float((unsigned)(267 - ex) << 23);
        kscale = 2.f * dt * __uint_as_float((unsigned)(ex - 28) << 23);
        C.r *= sc; C.i *= sc;
        const lptr st = lds + cs * LA_STG + lane * 8;
        if (hv == 0) {
          const cplx2 e1 = cexp2(al, wr, 1.f); cplx2 e16 = cexp2(al, wr, 16.f); e16.i = -e16.i;
          cplx2 y; y.r = (f32x2){32768.f, 32768.f}; y.i = (f32x2){0.f, 0.f};
          cplx2 x = C;
#pragma unroll
          for (int r = 0; r < 16; ++r) {
            put_row(st + r * 544, y); put_row(st + (16 + r) * 544, x);
            if (r < 15) { y = cmul(y, e16); x = cmul(x, e1); }
          }
        } else {
          const cplx2 e256 = cexp2(al, wr, 256.f), e2 = cexp2(al, wr, 2.f), e4 = cmul(e2, e2), e8 = cmul(e4, e4);
          cplx2 b = C; int row = 32;
#pragma unroll
          for (int t = 1; t < 8; ++t) {
            b = cmul(b, e256);
            const int n = t == 1 ? 9 : (t < 4 ? 5 : 3);
            cplx2 x = b;
#pragma unroll
            for (int r = 0; r < n; ++r) { put_row(st + (row + r) * 544, x); if (r + 1 < n) x = cmul(x, t == 1 ? e2 : (t < 4 ? e4 : e8)); }
            row += n;
          }
        }
      }
      lds_barrier();
      if (rd == 0) STAMP(1);
      f32x4 kt[4];
      if (act) {
        const lptr st = lds + cs * LA_STG + g * 16;
        const int r = 15 - s16;
        const int r2 = (r + ((r >> 1) & 1)) >> 1, r4 = (r + 1 + ((r >> 2) & 1)) >> 2, r8 = (r + 3 + ((r >> 3) & 1)) >> 3;
        lptr xa[4];
        xa[0] = st + (hv ? 51 + r8 : 16 + r) * 544; xa[1] = st + (hv ? 54 + r8 : 32 + r2) * 544;
        xa[2] = st + (hv ? 57 + r8 : 41 + r4) * 544; xa[3] = st + (hv ? 60 + r8 : 46 + r4) * 544;
#pragma unroll
        for (int i = 0; i < 4; ++i) kt[i] = (f32x4){0.f, 0.f, 0.f, 0.f};
#pragma unroll
        for (int ks = 0; ks < 8; ++ks) {
          const v4u Y = lds16(st + s16 * 544 + ks * 64);
#pragma unroll
          for (int i = 0; i < 4; ++i) kt[i] = mfma_h(lds16(xa[i] + ks * 64), Y, kt[i]);
        }
      }
      lds_barrier();
      if (rd == 1) stage_wo(g_wo, cg, lw1k, w, lane);
      if (rd == 0) STAMP(7);
      if (act) {
        const unsigned kb = (unsigned)(size_t)(lds + cs * LA_CH) + 16 + 2 * (2032 - 6 - 1024 * hv - 16 * s16 + 4 * g);
#pragma unroll
        for (int i = 0; i < 4; ++i) {
          f32x4 v = kt[i] * kscale;
          if (i == 0 && hv == 0 && lane == 48) v[3] += dskip;
          const unsigned d0 = pk_bf16(v[0], v[1]), d1 = pk_bf16(v[2], v[3]);
#pragma unroll
          for (int k = 0; k < 4; ++k) { const int kap = (k + (s16 >> 2)) & 3; ds_write2(kb + kap * (LA_KC + 4) - 512 * i, d0, d1); }
        }
        if (hv == 0) { const int k = g; if (s16 < 15 - k) *(LAS unsigned*)(lds + cs * LA_CH + k * LA_KC + 16 + 2 * (2042 + 2 * k) + s16 * 4) = 0u; }
        else if (lane < 56) *(LAS v4u*)(lds + cs * LA_CH + LA_U0 + (lane >= 28 ? LA_UC - 448 : 0) + lane * 16) = (v4u){0u, 0u, 0u, 0u};
      }
      if (rd == 0) asm volatile("s_waitcnt vmcnt(0)" : "+v"(ur[0]), "+v"(ur[1]), "+v"(ur[2]), "+v"(ur[3]), "+v"(uh), "+v"(pf[0]), "+v"(pf[1]) :: "memory");
#pragma unroll
      for (int c = 0; c < 4; ++c) {
        if (c < ns) {
          const int e = ush + (rd ? 4 : c), dw = e >> 1;
          const unsigned sel = (e & 1) ? 0x07060302u : 0x05040100u;
          auto pick = [&](const v3u& v) { return dw == 0 ? v.x : (dw == 1 ? v.y : v.z); };
          const unsigned p01 = __builtin_amdgcn_perm(pick(ur[1]), pick(ur[0]), sel), p23 = __builtin_amdgcn_perm(pick(ur[3]), pick(ur[2]), sel);
          const unsigned hq = hr < 0 ? 0u : __builtin_amdgcn_perm(pick(uh), 0u, sel);
          const unsigned q23 = __builtin_amdgcn_update_dpp(hq, p23, 0x138, 0xf, 0xf, false);
          const lptr up = lds + c * LA_CH + LA_U0 + 448 + rt * 8;
          *(LAS v2u*)(up) = (v2u){__builtin_amdgcn_alignbit(p01, q23, 16), __builtin_amdgcn_alignbit(p23, p01, 16)};
          *(LAS v2u*)(up + LA_UC) = (v2u){p01, p23};
        }
      }
      lds_barrier();
      if (rd == 0) STAMP(2);
      f32x4 y[8];
      if (act) {
#pragma unroll
        for (int t = 0; t < 8; ++t) y[t] = (f32x4){0.f, 0.f, 0.f, 0.f};
        const lptr kp = lds + cs * LA_CH + tp * LA_KC + 16 + 2 * (2040 - 1792 - 128 * hv + 8 * (g - tq));
        const lptr up = lds + cs * LA_CH + LA_U0 + (s16 & 1) * LA_UC + 2 * (224 - 128 * hv + 32 * (s16 >> 1) + 8 * g);
        v4u Kf[2][8], Uf[2][8];
#pragma unroll
        for (int d = 0; d < 8; ++d) { Kf[0][d] = lds16(kp + 512 * (7 - d)); Uf[0][d] = lds16(up + 512 * d); }
#pragma unroll
        for (int sg = 0; sg < 4; ++sg) {
          SCHED();
          if (sg < 3) {
#pragma unroll
            for (int d = 0; d < 8; ++d) { Kf[(sg + 1) & 1][d] = lds16(kp - 64 * (sg + 1) + 512 * (7 - d)); Uf[(sg + 1) & 1][d] = lds16(up - 64 * (sg + 1) + 512 * d); }
          }
          SCHED();
#pragma unroll
          for (int d = 0; d < 8; ++d)
#pragma unroll
            for (int t = d; t < 8; ++t) y[t] = mfma_bf(Uf[sg & 1][t - d], Kf[sg & 1][d], y[t]);
        }
        SCHED();
#pragma unroll
        for (int i = 0; i < 4; ++i) *(LAS f32x4*)(lds + (rd ? LA_STG : LA_RED) + w * 4096 + i * 1024 + lane * 16) = sel4(hv, y[i], y[4 + i]);
      }
      lds_barrier();
      if (rd == 0) STAMP(3);
      if (act) {
        const unsigned ob = (unsigned)(size_t)(lds + LA_OUT + cs * 4128) + 4 * (512 * hv + 32 * g + s16);
#pragma unroll
        for (int i = 0; i < 4; ++i) {
          const f32x4 v = sel4(hv, y[4 + i], y[i]) + *(LAS f32x4*)(lds + (rd ? LA_STG : LA_RED) + (w ^ 1) * 4096 + i * 1024 + lane * 16);
          asm volatile("ds_write2_b32 %0, %1, %2 offset1:16" :: "v"(ob + 512 * i), "v"(pk_bf16(v[0], v[1])), "v"(pk_bf16(v[2], v[3])) : "memory");
        }
      }
      lds_barrier();
      if (rd == 0 && nch == 4) stage_wo(g_wo, cg, lw1k, w, lane);
      {
        char* op = g_out + (size_t)(tid * 4) * 2304 + 2 * (c0 + (rd ? 4 : 0));
        const lptr ob = lds + LA_OUT + tid * 8;
        if (rd == 0) {
          const v2u a = *(LAS v2u*)(ob), b = *(LAS v2u*)(ob + 4128), c = *(LAS v2u*)(ob + 2 * 4128), d = *(LAS v2u*)(ob + 3 * 4128);
          const v2u r0 = {__builtin_amdgcn_perm(b.x, a.x, 0x05040100u), __builtin_amdgcn_perm(d.x, c.x, 0x05040100u)};
          const v2u r1 = {__builtin_amdgcn_perm(b.x, a.x, 0x07060302u), __builtin_amdgcn_perm(d.x, c.x, 0x07060302u)};
          const v2u r2 = {__builtin_amdgcn_perm(b.y, a.y, 0x05040100u), __builtin_amdgcn_perm(d.y, c.y, 0x05040100u)};
          const v2u r3 = {__builtin_amdgcn_perm(b.y, a.y, 0x07060302u), __builtin_amdgcn_perm(d.y, c.y, 0x07060302u)};
          asm volatile("global_store_dwordx2 %0, %1, off\n\tglobal_store_dwordx2 %0, %2, off offset:2304" :: "v"(op), "v"(r0), "v"(r1) : "memory");
          asm volatile("global_store_dwordx2 %0, %1, off\n\tglobal_store_dwordx2 %0, %2, off offset:2304" :: "v"(op + 4608), "v"(r2), "v"(r3) : "memory");
        } else {
          const v2u a = *(LAS v2u*)(ob);
          *(u16*)(op) = (u16)a.x; *(u16*)(op + 2304) = (u16)(a.x >> 16); *(u16*)(op + 4608) = (u16)a.y; *(u16*)(op + 6912) = (u16)(a.y >> 16);
        }
      }
      if (rd == 0) STAMP(4);
    };
    round(IC(0));
    if (nch == 5) round(IC(1));
    STAMP(5);
    DRAIN(); BAR(); asm volatile("" ::: "memory");
    STAMP(8);
  }

  {
    const int m = w & 3, kh = w >> 2, mt = xcd * 16 + (bi >> 3) * 4 + m;
    v4u wf[9][5];
    {
      const lptr wb = lds + L_WO + (288 * kh + 4 * g + tq) * 144 + tp * 8;
      const lptr ws = lds + L_WO + (288 * kh + 72 * g + 16 + tq) * 144 + tp * 8;
#pragma unroll
      for (int i = 0; i < 8; ++i)
#pragma unroll
        for (int n = 0; n < 5; ++n) wf[i][n] = trd2(wb + i * 36 * 144 + n * 32, wb + (i * 36 + 20) * 144 + n * 32);
#pragma unroll
      for (int n = 0; n < 5; ++n) wf[8][n] = trd2(ws + n * 32, ws + 36 * 144 + n * 32);
    }
    WAITCNT(63, 0);
    STAMP(9);
    if (w == 0) poll_flags(g_ws + WS_F2 + xcd * 128, 16, tag, lane);
    BAR(); asm volatile("" ::: "memory");
    STAMP(10);
    v4u of[9];
#pragma unroll
    for (int i = 0; i < 8; ++i) of[i] = __builtin_amdgcn_raw_buffer_load_b128(ws_rs, WS_OM + (mt * 16 + 8 * kh) * 1024 + lane * 16, i * 1024, 16);
    {
      const unsigned so = WS_OS + ((xcd * 16 + 8 * kh + 2 * g) * 256 + (mt & 15) * 16 + s16) * 8;
      const v2u a = __builtin_amdgcn_raw_buffer_load_b64(ws_rs, so, 0, 16), b = __builtin_amdgcn_raw_buffer_load_b64(ws_rs, so, 2048, 16);
      of[8] = (v4u){a.x, a.y, b.x, b.y};
    }
    f32x4 oc[5];
#pragma unroll
    for (int n = 0; n < 5; ++n) oc[n] = (f32x4){0.f, 0.f, 0.f, 0.f};
#pragma unroll
    for (int i = 0; i < 9; ++i)
#pragma unroll
      for (int n = 0; n < 5; ++n) oc[n] = mfma_bf(wf[i][n], of[i], oc[n]);
    if (kh) {
#pragma unroll
      for (int n = 0; n < 5; ++n) *(LAS f32x4*)(lds + L_RED + m * 5120 + n * 1024 + lane * 16) = oc[n];
    }
    lds_barrier();
    if (!kh) {
      const lptr sb = lds + L_OST + m * 16 * 176;
#pragma unroll
      for (int n = 0; n < 5; ++n) {
        const f32x4 v = oc[n] + *(LAS f32x4*)(lds + L_RED + m * 5120 + n * 1024 + lane * 16);
        *(LAS v2u*)(sb + s16 * 176 + n * 32 + g * 8) = (v2u){pk_bf16(v[0], v[1]), pk_bf16(v[2], v[3])};
      }
      asm volatile("" ::: "memory");
      char* ob = g_out + (size_t)mt * 16 * 2304 + 1152 + cg * 144;
#pragma unroll
      for (int j = 0; j < 3; ++j) {
        const unsigned c = j * 64 + lane, r = (c * 7282u) >> 16, k = c - r * 9u;
        if (c < 144) *(v4u*)(ob + r * 2304u + k * 16u) = lds16(sb + r * 176 + k * 16);
      }
    }
    if (blk == 0 && w == 4) {
#pragma unroll
      for (int j = 0; j < 4; ++j) poll_flags(g_ws + WS_F0 + j * 512, 64, tag, lane);
      if (lane == 0) flag_store(g_ws + WS_EPOCH, tag);
    }
  }
}

extern "C" void kernel_launch(void* const* d_in, const int* in_sizes, int n_in, void* d_out, int out_size, void* d_ws, size_t ws_size,
                              hipStream_t stream) {
  (void)in_sizes; (void)n_in; (void)out_size; (void)ws_size;
  (void)hipFuncSetAttribute((const void*)fused_kernel, hipFuncAttributeMaxDynamicSharedMemorySize, LDS_BYTES);
  fused_kernel<<<256, 512, LDS_BYTES, stream>>>((const char*)d_in[0], (const char*)d_in[1], (const char*)d_in[2], (const char*)d_in[3],
                                                (const char*)d_in[4], (const char*)d_in[5], (const char*)d_in[6], (const char*)d_in[7],
                                                (const char*)d_in[8], (const char*)d_in[9], (const char*)d_in[10], (char*)d_out, (char*)d_ws);
}
```

```cpp
#include <hip/hip_runtime.h>
#include <type_traits>

typedef unsigned short u16;
typedef unsigned long long u64;
typedef unsigned v4u __attribute__((ext_vector_type(4)));
typedef unsigned v3u __attribute__((ext_vector_type(3)));
typedef unsigned v2u __attribute__((ext_vector_type(2)));
typedef float f32x4 __attribute__((ext_vector_type(4)));
typedef float f32x2 __attribute__((ext_vector_type(2)));
typedef short s16x4 __attribute__((ext_vector_type(4)));
typedef __bf16 bf16x8 __attribute__((ext_vector_type(8)));
typedef __bf16 bf16x2 __attribute__((ext_vector_type(2)));
typedef _Float16 f16x8 __attribute__((ext_vector_type(8)));
typedef _Float16 f16x2 __attribute__((ext_vector_type(2)));
#define LAS __attribute__((address_space(3)))
#define GAS __attribute__((address_space(1)))
#define DI __device__ __forceinline__
typedef LAS char* lptr;

constexpr int WS_EPOCH = 0;
constexpr int WS_F0 = 256;
constexpr int WS_F1 = 4096;
constexpr int WS_F2 = 8192;
constexpr int WS_PART = 16384;
constexpr int PART_B = 5632;
constexpr int WS_OM = 1 << 20;
constexpr int WS_OS = 3 << 20;
constexpr int WS_DBG = 4 << 20;
constexpr int WS_USED = 5 << 20;
constexpr int LDS_BYTES = 160 * 1024;

constexpr int LB_W = 0, LB_WSLOT = 24576, LB_X = 73728, LB_WPHI = 141312;
constexpr int LB_IMG = 0, L_WO = 57344, LB_KVF = 143360;
constexpr int L_RED = 0, L_OST = 20480;
constexpr int LA_STG = 34272;
constexpr int LA_KC = 4160, LA_UC = 4640, LA_U0 = 4 * LA_KC, LA_CH = 4 * LA_KC + 2 * LA_UC;
constexpr int LA_RED = 103680;
constexpr int LA_OUT = 140544;

#define PROFW 0
#define STAMP(i) do {} while (0)

DI float bf2f(unsigned lo16) { return __uint_as_float(lo16 << 16); }
DI unsigned pk_bf16(float a, float b) { f32x2 v = {a, b}; return __builtin_bit_cast(unsigned, __builtin_convertvector(v, bf16x2)); }
DI unsigned pk_f16(float a, float b) { f32x2 v = {a, b}; return __builtin_bit_cast(unsigned, __builtin_convertvector(v, f16x2)); }
DI float rnd_bf16(float a) { return bf2f(pk_bf16(a, a) & 0xffffu); }
DI f32x4 mfma_bf(v4u a, v4u b, f32x4 c) { return __builtin_amdgcn_mfma_f32_16x16x32_bf16(__builtin_bit_cast(bf16x8, a), __builtin_bit_cast(bf16x8, b), c, 0, 0, 0); }
DI f32x4 mfma_h(v4u a, v4u b, f32x4 c) { return __builtin_amdgcn_mfma_f32_16x16x32_f16(__builtin_bit_cast(f16x8, a), __builtin_bit_cast(f16x8, b), c, 0, 0, 0); }
DI v2u trd(lptr p) { return __builtin_bit_cast(v2u, __builtin_amdgcn_ds_read_tr16_b64_v4i16((LAS s16x4*)p)); }
DI v4u trd2(lptr p0, lptr p1) { v2u a = trd(p0), b = trd(p1); return (v4u){a.x, a.y, b.x, b.y}; }
DI v4u lds16(lptr p) { return *(LAS v4u*)p; }
template <int IMM> DI void glds(const char* base, unsigned voff, unsigned lbase) {
  asm volatile("s_add_u32 m0, %2, %3\n\ts_nop 0\n\tglobal_load_lds_dwordx4 %0, %1" :: "v"(voff), "s"(base), "s"(lbase), "i"(IMM) : "memory", "scc");
}
#define IC(n) std::integral_constant<int, (n)>{}
template <int N, int I = 0, class F> DI void static_for(F&& f) { if constexpr (I < N) { f(std::integral_constant<int, I>{}); static_for<N, I + 1>(f); } }
DI void gld32(unsigned& d, const char* base, unsigned voff) { asm volatile("global_load_dword %0, %1, %2" : "=v"(d) : "v"(voff), "s"(base) : "memory"); }
DI void gld16(unsigned& d, const char* base, unsigned voff) { asm volatile("global_load_ushort %0, %1, %2" : "=v"(d) : "v"(voff), "s"(base) : "memory"); }
DI void gld96(v3u& d, const char* base, unsigned voff) { asm volatile("global_load_dwordx3 %0, %1, %2" : "=v"(d) : "v"(voff), "s"(base) : "memory"); }
#define WAITCNT(vm, lgkm) __builtin_amdgcn_s_waitcnt(((vm) & 15) | (((vm) >> 4) << 14) | 0x70 | ((lgkm) << 8))
#define BAR() __builtin_amdgcn_s_barrier()
#define SCHED() __builtin_amdgcn_sched_barrier(0)
#define DRAIN() asm volatile("s_waitcnt vmcnt(0)" ::: "memory")
DI void lds_barrier() { asm volatile("" ::: "memory"); WAITCNT(63, 0); BAR(); asm volatile("" ::: "memory"); }
DI f32x4 sel4(bool c, f32x4 a, f32x4 b) { return (f32x4){c ? a[0] : b[0], c ? a[1] : b[1], c ? a[2] : b[2], c ? a[3] : b[3]}; }
DI void ds_write2(unsigned addr, unsigned a, unsigned b) { asm volatile("ds_write2_b32 %0, %1, %2 offset1:1" :: "v"(addr), "v"(a), "v"(b) : "memory"); }

DI u64 mix64(u64 z) { z += 0x9E3779B97F4A7C15ull; z = (z ^ (z >> 30)) * 0xBF58476D1CE4E5B9ull; z = (z ^ (z >> 27)) * 0x94D049BB133111EBull; return z ^ (z >> 31); }
DI u64 read_tag(const char* ws) {
  u64 e; asm volatile("s_load_dwordx2 %0, %1, 0x0\n\ts_waitcnt lgkmcnt(0)" : "=s"(e) : "s"(ws) : "memory");
  return mix64(e);
}
DI void flag_store(char* p, u64 tag) { __hip_atomic_store((GAS u64*)p, tag, __ATOMIC_RELAXED, __HIP_MEMORY_SCOPE_AGENT); }
DI void poll_flags(char* p, int n, u64 tag, int lane) {
  GAS u64* f = (GAS u64*)p + (lane < n ? lane : 0);
  for (unsigned spins = 0; spins < (1u << 22); ++spins) {
    u64 v = __hip_atomic_load(f, __ATOMIC_RELAXED, __HIP_MEMORY_SCOPE_AGENT);
    if (__all(v == tag)) break;
    __builtin_amdgcn_s_sleep(1);
  }
  __builtin_amdgcn_fence(__ATOMIC_ACQUIRE, "wavefront");
}

DI void swap32(float& a, float& b) { auto r = __builtin_amdgcn_permlane32_swap(__float_as_uint(a), __float_as_uint(b), false, false); a = __uint_as_float(r[0]); b = __uint_as_float(r[1]); }
DI void swap16(float& a, float& b) { auto r = __builtin_amdgcn_permlane16_swap(__float_as_uint(a), __float_as_uint(b), false, false); a = __uint_as_float(r[0]); b = __uint_as_float(r[1]); }

DI void features(const f32x4& F0, const f32x4& F1, float e, v4u& P0, v4u& P1, unsigned& PX) {
  const float L = 1.4426950408889634f;
  float a = fmaxf(fmaxf(F0[0], F0[1]), fmaxf(F0[2], F0[3])), b = fmaxf(fmaxf(F1[0], F1[1]), fmaxf(F1[2], F1[3]));
  float c = -fminf(fminf(F0[0], F0[1]), fminf(F0[2], F0[3])), d = -fminf(fminf(F1[0], F1[1]), fminf(F1[2], F1[3]));
  swap32(a, b); swap32(c, d);
  float r1 = fmaxf(fmaxf(a, b), e), r2 = fmaxf(fmaxf(c, d), -e);
  swap16(r1, r2);
  float t1 = fmaxf(r1, r2) * -L, t2 = t1;
  swap16(t1, t2);
  float m0 = t1, m1 = t1, n0 = t2, n1 = t2;
  swap32(m0, m1); swap32(n0, n1);
  float p0[4], q0[4], p1[4], q1[4];
#pragma unroll
  for (int j = 0; j < 4; ++j) {
    p0[j] = __builtin_amdgcn_exp2f(fmaf(F0[j], L, m0)); q0[j] = __builtin_amdgcn_exp2f(fmaf(F0[j], -L, n0));
    p1[j] = __builtin_amdgcn_exp2f(fmaf(F1[j], L, m1)); q1[j] = __builtin_amdgcn_exp2f(fmaf(F1[j], -L, n1));
  }
  float px = __builtin_amdgcn_exp2f(fmaf(e, L, t1)), qx = __builtin_amdgcn_exp2f(fmaf(e, -L, t2));
  a = (p0[0] + p0[1]) + (p0[2] + p0[3]); b = (p1[0] + p1[1]) + (p1[2] + p1[3]);
  c = (q0[0] + q0[1]) + (q0[2] + q0[3]); d = (q1[0] + q1[1]) + (q1[2] + q1[3]);
  swap32(a, b); swap32(c, d);
  r1 = a + b + px; r2 = c + d + qx;
  swap16(r1, r2);
  t1 = __builtin_amdgcn_rcpf(r1 + r2); t2 = t1;
  swap16(t1, t2);
  m0 = t1; m1 = t1; n0 = t2; n1 = t2;
  swap32(m0, m1); swap32(n0, n1);
  P0 = (v4u){pk_bf16(p0[0] * m0, p0[1] * m0), pk_bf16(p0[2] * m0, p0[3] * m0), pk_bf16(q0[0] * n0, q0[1] * n0), pk_bf16(q0[2] * n0, q0[3] * n0)};
  P1 = (v4u){pk_bf16(p1[0] * m1, p1[1] * m1), pk_bf16(p1[2] * m1, p1[3] * m1), pk_bf16(q1[0] * n1, q1[1] * n1), pk_bf16(q1[2] * n1, q1[3] * n1)};
  PX = pk_bf16(px * t1, qx * t2);
}

struct cplx2 { f32x2 r, i; };
DI cplx2 cmul(const cplx2& a, const cplx2& b) { cplx2 c; c.r = a.r * b.r - a.i * b.i; c.i = a.r * b.i + a.i * b.r; return c; }
DI cplx2 cexp2(f32x2 al, f32x2 wr, float p) {
  cplx2 c;
#pragma unroll
  for (int k = 0; k < 2; ++k) {
    float m = __builtin_amdgcn_exp2f(al[k] * p), f = __builtin_amdgcn_fractf(wr[k] * p);
    c.r[k] = m * __builtin_amdgcn_cosf(f); c.i[k] = m * __builtin_amdgcn_sinf(f);
  }
  return c;
}
DI void put_row(lptr p, const cplx2& v) { *(LAS v2u*)p = (v2u){pk_f16(v.r[0], v.i[0]), pk_f16(v.r[1], v.i[1])}; }

DI void stage_wo(const char* wo, int cg, unsigned lw1k, int w, int lane) {
  int ln = lane; asm volatile("" : "+v"(ln));
  const char* b = wo + cg * 144;
  static_for<11>([&](auto I) {
    constexpr int i = I;
    if (i < 10 || w == 0) {
      const unsigned c = (i * 8 + w) * 64 + ln, r = (c * 7282u) >> 16;
      glds<L_WO + i * 8192>(b, r * 1008u + c * 16u, lw1k);
    }
  });
}

__global__ __launch_bounds__(512, 2) void fused_kernel(
    const char* __restrict__ g_u, const char* __restrict__ g_lar, const char* __restrict__ g_aim, const char* __restrict__ g_cr,
    const char* __restrict__ g_ci, const char* __restrict__ g_ldt, const char* __restrict__ g_dsk, const char* __restrict__ g_x,
    const char* __restrict__ g_w, const char* __restrict__ g_wphi, const char* __restrict__ g_wo, char* __restrict__ g_out, char* g_ws) {
  extern __shared__ __attribute__((aligned(16))) char smem[];
  const lptr lds = (lptr)smem;
  const int tid = threadIdx.x, lane = tid & 63, w = __builtin_amdgcn_readfirstlane(tid >> 6) & 7;
  const int blk = blockIdx.x, xcd = blk & 7, bi = blk >> 3;
  const int g = lane >> 4, s16 = lane & 15, tq = (lane >> 2) & 3, tp = lane & 3;
  const int cg = bi & 7;
  const unsigned lw1k = (unsigned)(size_t)lds + w * 1024;
  STAMP(0);
  const u64 pad_t0 = __builtin_amdgcn_s_memrealtime();
  asm volatile("" :: "s"(g_u), "s"(g_lar), "s"(g_aim), "s"(g_cr), "s"(g_ci), "s"(g_ldt), "s"(g_dsk), "s"(g_x), "s"(g_w), "s"(g_wphi), "s"(g_wo), "s"(g_out), "s"(g_ws));
  const __amdgpu_buffer_rsrc_t ws_rs = __builtin_amdgcn_make_buffer_rsrc(g_ws, 0, WS_USED, 0x00020000);
  u64 tag;

  if (bi < 16) {
    const int rb = (xcd >> 1) * 2 + (bi >> 3), hp = (xcd & 1) * 8 + (bi & 7);
    const int lr = lane >> 3, lc = lane & 7;
    const int wrow = 8 * w + lr;
    unsigned w_off = wrow * 3456u + (((72 * hp) & ~63) + ((lc ^ ((wrow & 2) | ((wrow >> 1) & 4))) << 4));
    const char* xb0 = g_x + (size_t)(rb * 256 + w * 32) * 1152;
    const char* xb1 = xb0 + 16 * 1152;
    const char *wb1 = g_w + 1152, *wb2 = g_w + 2304;
    unsigned x_off0 = lr * 1152u + ((lc ^ (lane >> 4)) << 4), x_off1 = (8 + lr) * 1152u + ((lc ^ (4 + (lane >> 4))) << 4);
    const unsigned lw8k = (unsigned)(size_t)lds + w * 8192;
    auto issue_w = [&](auto T) {
      constexpr int o = LB_W + (T % 3) * LB_WSLOT;
      glds<o>(g_w, w_off, lw1k); glds<o + 8192>(wb1, w_off, lw1k); glds<o + 16384>(wb2, w_off, lw1k);
      w_off += 64 * 3456; asm volatile("" : "+v"(w_off));
    };
    auto issue_x = [&](auto T) {
      constexpr int o = LB_X + (T & 1) * 4096;
      glds<o>(xb0, x_off0, lw8k); glds<o + 1024>(xb0, x_off1, lw8k); glds<o + 2048>(xb1, x_off0, lw8k); glds<o + 3072>(xb1, x_off1, lw8k);
      x_off0 += 128; x_off1 += 128; asm volatile("" : "+v"(x_off0), "+v"(x_off1));
    };
    issue_w(IC(0)); issue_x(IC(0));
    if (w < 2) { const int c = w * 64 + lane; glds<LB_WPHI>(g_wphi + hp * 1296, (c < 81 ? c : 80) * 16, lw1k); }
    issue_w(IC(1)); issue_x(IC(1)); issue_w(IC(2));
    tag = read_tag(g_ws);
    const int ub = ((72 * hp) & 63) >> 3, fsw = (tq & 2) | ((g & 1) << 2), trow = (8 * g + tq) * 128;
    const lptr wa0 = lds + trow + ((((ub + tp) >> 1) ^ fsw) << 4) + ((ub + tp) & 1) * 8;
    const lptr wa1 = lds + trow + ((((ub + 5 + tp) >> 1) ^ fsw) << 4) + ((ub + 5 + tp) & 1) * 8;
    const lptr was = lds + trow + ((((ub + 4) >> 1) ^ fsw) << 4) + ((ub + 4) & 1) * 8 + (tp < 2 ? tp : 2) * 8192;
    const lptr xa0 = lds + LB_X + w * 8192 + s16 * 128 + ((g ^ (s16 >> 1)) << 4);
    const lptr xa1 = lds + LB_X + w * 8192 + s16 * 128 + (((4 + g) ^ (s16 >> 1)) << 4);
    auto read_w = [&](v4u (&f)[7], int t, int ks) {
      const int o = (t % 3) * LB_WSLOT + ks * 4096;
#pragma unroll
      for (int p = 0; p < 3; ++p) {
        f[2 * p] = trd2(wa0 + o + p * 8192, wa0 + o + p * 8192 + 512);
        f[2 * p + 1] = trd2(wa1 + o + p * 8192, wa1 + o + p * 8192 + 512);
      }
      f[6] = trd2(was + o, was + o + 512);
    };
    auto read_x = [&](v4u (&f)[4], int t) {
      const int o = (t & 1) * 4096;
      f[0] = lds16(xa0 + o); f[1] = lds16(xa0 + o + 2048); f[2] = lds16(xa1 + o); f[3] = lds16(xa1 + o + 2048);
    };
    f32x4 acc[7][2];
#pragma unroll
    for (int n = 0; n < 7; ++n) { acc[n][0] = (f32x4){0.f, 0.f, 0.f, 0.f}; acc[n][1] = acc[n][0]; }
    v4u wf0[7], wf1[7], xc[4], xn[4];
    unsigned wm[4][2], wsp[4];
    WAITCNT(10, 15); BAR(); asm volatile("" ::: "memory");
    STAMP(1);
    if (tid == 0) flag_store(g_ws + WS_F0 + blk * 8, tag);
    read_x(xc, 0); read_w(wf0, 0, 0);
    WAITCNT(63, 0); asm volatile("" ::: "memory");
    issue_x(IC(2));
    static_for<9>([&](auto T) {
      constexpr int t = T;
      SCHED();
      read_w(wf1, t, 1);
      if (t == 2) {
        const lptr wp = lds + LB_WPHI;
#pragma unroll
        for (int fi = 0; fi < 4; ++fi) {
          const int hh = fi & 1, f = fi < 2 ? s16 : 16 + ((s16 >> 2) & 1);
          const lptr b = wp + hh * 648 + f * 2;
          const int d0 = 4 * g + 2 * hh, ds = hh ? 0 : 16;
          unsigned e0 = *(LAS u16*)(b + d0 * 36), e1 = *(LAS u16*)(b + d0 * 36 + 36), e2 = *(LAS u16*)(b + d0 * 36 + 72), e3 = *(LAS u16*)(b + d0 * 36 + 108);
          unsigned e4 = *(LAS u16*)(b + ds * 36), e5 = *(LAS u16*)(b + ds * 36 + 36);
          const bool z = fi >= 2 && (s16 >> 3) != hh;
          wm[fi][0] = z ? 0u : (e0 | (e1 << 16)); wm[fi][1] = z ? 0u : (e2 | (e3 << 16)); wsp[fi] = z ? 0u : (e4 | (e5 << 16));
        }
      }
      SCHED();
#pragma unroll
      for (int n = 0; n < 7; ++n) { acc[n][0] = mfma_bf(wf0[n], xc[0], acc[n][0]); acc[n][1] = mfma_bf(wf0[n], xc[1], acc[n][1]); }
      SCHED();
      if (t < 7) WAITCNT(7, 0); else WAITCNT(0, 0);
      BAR(); asm volatile("" ::: "memory");
      if constexpr (t < 6) issue_w(IC(t + 3));
      if (t == 8) stage_wo(g_wo, cg, lw1k, w, lane);
      SCHED();
      if (t < 8) { read_x(xn, t + 1); read_w(wf0, t + 1, 0); }
      SCHED();
#pragma unroll
      for (int n = 0; n < 7; ++n) { acc[n][0] = mfma_bf(wf1[n], xc[2], acc[n][0]); acc[n][1] = mfma_bf(wf1[n], xc[3], acc[n][1]); }
      SCHED();
      if (t < 8) {
        WAITCNT(63, 0); asm volatile("" ::: "memory");
        if constexpr (t < 6) issue_x(IC(t + 3));
#pragma unroll
        for (int i = 0; i < 4; ++i) xc[i] = xn[i];
      }
    });
    STAMP(2);
    auto phi = [&](int part, int m, v4u& P0, v4u& P1, unsigned& PX) {
      const f32x4 &T0 = acc[2 * part][m], &T1 = acc[2 * part + 1][m], &S = acc[6][m];
      const unsigned s0 = pk_bf16(S[0], S[1]), s1 = pk_bf16(S[2], S[3]);
      const v4u B0 = {pk_bf16(T0[0], T0[1]), pk_bf16(T0[2], T0[3]), s0, s1}, B1 = {pk_bf16(T1[0], T1[1]), pk_bf16(T1[2], T1[3]), s0, s1};
      const bool on = g == part;
      const f32x4 z = {0.f, 0.f, 0.f, 0.f};
      const f32x4 F0 = mfma_bf((v4u){wm[0][0], wm[0][1], on ? wsp[0] : 0u, 0u}, B0, z);
      const f32x4 F1 = mfma_bf((v4u){wm[1][0], wm[1][1], 0u, on ? wsp[1] : 0u}, B1, z);
      f32x4 F2 = mfma_bf((v4u){wm[2][0], wm[2][1], on ? wsp[2] : 0u, 0u}, B0, z);
      F2 = mfma_bf((v4u){wm[3][0], wm[3][1], 0u, on ? wsp[3] : 0u}, B1, F2);
      features(F0, F1, F2[0], P0, P1, PX);
    };
    {
      const int sw = s16 >> 2;
#pragma unroll
      for (int m = 0; m < 2; ++m) {
        v4u P0, P1; unsigned PX;
        phi(1, m, P0, P1, PX);
        const lptr row = lds + LB_IMG + (32 * w + 16 * m + s16) * 224, rp = row + ((g ^ sw) << 3);
        *(LAS v2u*)(rp) = (v2u){P0.x, P0.y}; *(LAS v2u*)(rp + 32) = (v2u){P0.z, P0.w};
        *(LAS v2u*)(rp + 64) = (v2u){P1.x, P1.y}; *(LAS v2u*)(rp + 96) = (v2u){P1.z, P1.w};
        *(LAS unsigned*)(row + 128 + (((g >> 1) ^ sw) << 3) + (g & 1) * 4) = PX;
        const f32x4 &V0 = acc[4][m], &V1 = acc[5][m], &S = acc[6][m];
        *(LAS v2u*)(rp + 160) = (v2u){pk_bf16(V0[0], V0[1]), pk_bf16(V0[2], V0[3])};
        *(LAS v2u*)(rp + 192) = (v2u){pk_bf16(V1[0], V1[1]), pk_bf16(V1[2], V1[3])};
        if (g >= 2) *(LAS v2u*)(rp + 128) = g == 2 ? (v2u){pk_bf16(S[0], S[1]), pk_bf16(S[2], S[3])} : (v2u){0x3f803f80u, 0x3f803f80u};
      }
    }
    lds_barrier();
    STAMP(3);
    {
      const int a = w & 3, h = a >> 1;
      const lptr ib = lds + LB_IMG + (4 * g + tq) * 224 + ((tp ^ g) << 3);
      const lptr pa = ib + a * 32, pb = ib + (w < 4 ? 160 + 32 * h : 128), pe = ib + 128;
      const bool ex = (w & 5) == 0 || w == 4;
      f32x4 c0 = {0.f, 0.f, 0.f, 0.f}, c1 = c0;
#pragma unroll
      for (int ks = 0; ks < 8; ++ks) {
        const int o = ks * 32 * 224;
        const v4u A = trd2(pa + o, pa + o + 16 * 224), B = trd2(pb + o, pb + o + 16 * 224);
        c0 = mfma_bf(A, B, c0);
        if (ex) c1 = mfma_bf(trd2(pe + o, pe + o + 16 * 224), B, c1);
      }
      const unsigned pbase = WS_PART + (hp * 8 + rb) * PART_B;
      if (w < 4) {
        __builtin_amdgcn_raw_buffer_store_b128(__builtin_bit_cast(v4u, c0), ws_rs, pbase + (a * 64 + lane) * 16, 0, 16);
        if (ex && g == h) __builtin_amdgcn_raw_buffer_store_b128(__builtin_bit_cast(v4u, c1), ws_rs, pbase + (304 + h * 16 + s16) * 16, 0, 16);
      } else {
        const int rec = s16 == 12 ? 0 : s16 - 7 - 2 * h;
        if (s16 == 12 || rec == 1 || rec == 2) __builtin_amdgcn_raw_buffer_store_b128(__builtin_bit_cast(v4u, c0), ws_rs, pbase + (256 + (a * 3 + rec) * 4 + g) * 16, 0, 16);
        const int rx = s16 == 12 ? 0 : s16 - 7 - 2 * g;
        if (ex && g < 2 && (s16 == 12 || rx == 1 || rx == 2)) __builtin_amdgcn_raw_buffer_store_b128(__builtin_bit_cast(v4u, c1), ws_rs, pbase + (336 + g * 3 + rx) * 16, 0, 16);
      }
    }
    DRAIN(); BAR(); asm volatile("" ::: "memory");
    if (tid == 0) flag_store(g_ws + WS_F1 + hp * 128 + rb * 8, tag);
    STAMP(4);
    v4u Q0[2], Q1[2]; unsigned QX[2];
    phi(0, 0, Q0[0], Q1[0], QX[0]); phi(0, 1, Q0[1], Q1[1], QX[1]);
    STAMP(5);
    if (w == 0) poll_flags(g_ws + WS_F1 + hp * 128, 8, tag, lane);
    BAR(); asm volatile("" ::: "memory");
    STAMP(6);
    if (tid < 342) {
      v4u pv[8];
#pragma unroll
      for (int r = 0; r < 8; ++r) pv[r] = __builtin_amdgcn_raw_buffer_load_b128(ws_rs, WS_PART + hp * 8 * PART_B + tid * 16, r * PART_B, 16);
      f32x4 sm = __builtin_bit_cast(f32x4, pv[0]);
#pragma unroll
      for (int r = 1; r < 8; ++r) sm += __builtin_bit_cast(f32x4, pv[r]);
      int dst;
      if (tid < 256) dst = (tid >> 7) * 1024 + (tid & 63) * 16 + ((tid >> 6) & 1) * 8;
      else if (tid < 304) { const int i = tid - 256, a = i / 12, rec = (i >> 2) - a * 3; dst = 2048 + (a >> 1) * 256 + rec * 64 + (i & 3) * 16 + (a & 1) * 8; }
      else if (tid < 336) dst = 2560 + (tid - 304) * 8;
      else { const int i = tid - 336; dst = 2816 + (i < 3 ? i : i + 1) * 8; }
      *(LAS v2u*)(lds + LB_KVF + dst) = (v2u){pk_bf16(sm[0], sm[1]), pk_bf16(sm[2], sm[3])};
    }
    lds_barrier();
    STAMP(7);
    {
      const lptr kb = lds + LB_KVF;
      const int rec = tp < 3 ? tp : 0;
      v4u Nm[2], Em[2]; unsigned Nx[2], Ex[2];
#pragma unroll
      for (int h = 0; h < 2; ++h) {
        Nm[h] = lds16(kb + h * 1024 + lane * 16); Em[h] = lds16(kb + 2048 + h * 256 + rec * 64 + g * 16);
        Nx[h] = *(LAS unsigned*)(kb + 2560 + h * 128 + s16 * 8 + (g & 1) * 4); Ex[h] = *(LAS unsigned*)(kb + 2816 + h * 32 + rec * 8 + (g & 1) * 4);
      }
      v2u sp[2];
#pragma unroll
      for (int m = 0; m < 2; ++m) {
        f32x4 O[2], E[2];
#pragma unroll
        for (int h = 0; h < 2; ++h) {
          const v4u Bm = h ? Q1[m] : Q0[m], Bx = {(g >> 1) == h ? QX[m] : 0u, 0u, 0u, 0u};
          const f32x4 z = {0.f, 0.f, 0.f, 0.f};
          O[h] = mfma_bf((v4u){Nx[h], 0u, 0u, 0u}, Bx, mfma_bf(Nm[h], Bm, z));
          E[h] = mfma_bf((v4u){Ex[h], 0u, 0u, 0u}, Bx, mfma_bf(Em[h], Bm, z));
        }
        const float r0 = __builtin_amdgcn_rcpf(E[0][0] + 1e-6f), r1 = __builtin_amdgcn_rcpf(E[1][0] + 1e-6f);
        const v4u ov = {pk_bf16(O[0][0] * r0, O[0][1] * r0), pk_bf16(O[0][2] * r0, O[0][3] * r0), pk_bf16(O[1][0] * r1, O[1][1] * r1), pk_bf16(O[1][2] * r1, O[1][3] * r1)};
        __builtin_amdgcn_raw_buffer_store_b128(ov, ws_rs, WS_OM + ((rb * 16 + 2 * w + m) * 16 + hp) * 1024 + lane * 16, 0, 16);
        sp[m] = (v2u){pk_bf16(E[0][1] * r0, E[0][2] * r0), pk_bf16(E[1][1] * r1, E[1][2] * r1)};
      }
      if (g < 2) __builtin_amdgcn_raw_buffer_store_b64((v2u){g ? sp[1].x : sp[0].x, g ? sp[1].y : sp[0].y}, ws_rs, WS_OS + ((rb * 16 + hp) * 256 + 32 * w + lane) * 8, 0, 16);
    }
    DRAIN(); BAR(); asm volatile("" ::: "memory");
    if (tid == 0) flag_store(g_ws + WS_F2 + rb * 128 + hp * 8, tag);
    STAMP(8);
  } else {
    const int ab = xcd * 16 + (bi - 16);
    const int c0 = 9 * (ab >> 1) + ((ab & 1) ? 5 : 0), nch = (ab & 1) ? 4 : 5;
    const int cs = w >> 1, hv = w & 1;
    unsigned prm[2][4]; unsigned pdt[2], pds[2];
#pragma unroll
    for (int r = 0; r < 2; ++r) {
      const int ch = r ? c0 + nch - 1 : c0 + cs;
      const unsigned o = ch * 256 + lane * 4;
      gld32(prm[r][0], g_lar, o); gld32(prm[r][1], g_aim, o); gld32(prm[r][2], g_cr, o); gld32(prm[r][3], g_ci, o);
      gld16(pdt[r], g_ldt, ch * 2); gld16(pds[r], g_dsk, ch * 2);
    }
    unsigned pf[2];
#pragma unroll
    for (int j = 0; j < 2; ++j) {
      unsigned i = j * 8192 + (bi - 16) * 512 + tid; if (i > 13247) i = 13247;
      const unsigned kt = i / 1472u, r = i - kt * 1472u;
      const unsigned r2 = r - 512u, row = r2 / 15u, q = r2 - row * 15u, part = q / 5u;
      const char* p = r < 512u ? g_x + ((xcd >> 1) * 512 + r) * 1152u + kt * 128u
                               : g_w + (kt * 64u + row) * 3456u + part * 1152u + ((xcd & 1) * 4 + q - part * 5u) * 128u;
      asm volatile("global_load_dword %0, %1, off" : "=v"(pf[j]) : "v"(p) : "memory");
    }
    int ua = (2 * c0) & ~3; if (ua > 1140) ua = 1140;
    const int ush = c0 - (ua >> 1);
    const int rt = (tid + (bi - 16) * 64) & 511;
    v3u ur[4], uh;
#pragma unroll
    for (int j = 0; j < 4; ++j) gld96(ur[j], g_u + ua, (rt * 4 + j) * 1152u);
    const int hr = (rt & ~63) * 4 - 1;
    gld96(uh, g_u + ua, (hr < 0 ? 0 : hr) * 1152u);
    tag = read_tag(g_ws);
    asm volatile("s_waitcnt vmcnt(7)" : "+v"(prm[0][0]), "+v"(prm[0][1]), "+v"(prm[0][2]), "+v"(prm[0][3]), "+v"(prm[1][0]), "+v"(prm[1][1]), "+v"(prm[1][2]),
                 "+v"(prm[1][3]), "+v"(pdt[0]), "+v"(pdt[1]), "+v"(pds[0]), "+v"(pds[1]) :: "memory");
    STAMP(6);
    if (tid == 0) flag_store(g_ws + WS_F0 + blk * 8, tag);

    auto round = [&](auto RD) {
      constexpr int rd = RD;
      const bool act = rd == 0 || w < 2;
      const int ns = rd == 0 ? 4 : 1;
      float kscale = 0.f, dskip = 0.f;
      if (act) {
        const unsigned q0 = rd ? prm[1][0] : prm[0][0], q1 = rd ? prm[1][1] : prm[0][1], q2 = rd ? prm[1][2] : prm[0][2], q3 = rd ? prm[1][3] : prm[0][3];
        const float dt = rnd_bf16(expf(bf2f(rd ? pdt[1] : pdt[0])));
        dskip = bf2f(rd ? pds[1] : pds[0]);
        f32x2 al, wr; cplx2 C;
        al[0] = -rnd_bf16(expf(bf2f(q0 & 0xffffu))) * dt * 1.4426950408889634f; al[1] = -rnd_bf16(expf(bf2f(q0 >> 16))) * dt * 1.4426950408889634f;
        wr[0] = bf2f(q1 & 0xffffu) * dt * 0.15915494309189535f; wr[1] = bf2f(q1 >> 16) * dt * 0.15915494309189535f;
        C.r[0] = bf2f(q2 & 0xffffu); C.r[1] = bf2f(q2 >> 16); C.i[0] = bf2f(q3 & 0xffffu); C.i[1] = bf2f(q3 >> 16);
        float mx = fmaxf(fmaxf(fabsf(C.r[0]), fabsf(C.r[1])), fmaxf(fabsf(C.i[0]), fabsf(C.i[1])));
#pragma unroll
        for (int o = 32; o >= 1; o >>= 1) mx = fmaxf(mx, __shfl_xor(mx, o));
        int ex = (int)((__float_as_uint(mx) >> 23) & 255u); ex = ex < 30 ? 30 : (ex > 230 ? 230 : ex);
        const float sc = __uint_as_float((unsigned)(267 - ex) << 23);
        kscale = 2.f * dt * __uint_as_float((unsigned)(ex - 28) << 23);
        C.r *= sc; C.i *= sc;
        const lptr st = lds + cs * LA_STG + lane * 8;
        if (hv == 0) {
          const cplx2 e1 = cexp2(al, wr, 1.f); cplx2 e16 = cexp2(al, wr, 16.f); e16.i = -e16.i;
          cplx2 y; y.r = (f32x2){32768.f, 32768.f}; y.i = (f32x2){0.f, 0.f};
          cplx2 x = C;
#pragma unroll
          for (int r = 0; r < 16; ++r) {
            put_row(st + r * 544, y); put_row(st + (16 + r) * 544, x);
            if (r < 15) { y = cmul(y, e16); x = cmul(x, e1); }
          }
        } else {
          const cplx2 e256 = cexp2(al, wr, 256.f), e2 = cexp2(al, wr, 2.f), e4 = cmul(e2, e2), e8 = cmul(e4, e4);
          cplx2 b = C; int row = 32;
#pragma unroll
          for (int t = 1; t < 8; ++t) {
            b = cmul(b, e256);
            const int n = t == 1 ? 9 : (t < 4 ? 5 : 3);
            cplx2 x = b;
#pragma unroll
            for (int r = 0; r < n; ++r) { put_row(st + (row + r) * 544, x); if (r + 1 < n) x = cmul(x, t == 1 ? e2 : (t < 4 ? e4 : e8)); }
            row += n;
          }
        }
      }
      lds_barrier();
      if (rd == 0) STAMP(1);
      f32x4 kt[4];
      if (act) {
        const lptr st = lds + cs * LA_STG + g * 16;
        const int r = 15 - s16;
        const int r2 = (r + ((r >> 1) & 1)) >> 1, r4 = (r + 1 + ((r >> 2) & 1)) >> 2, r8 = (r + 3 + ((r >> 3) & 1)) >> 3;
        lptr xa[4];
        xa[0] = st + (hv ? 51 + r8 : 16 + r) * 544; xa[1] = st + (hv ? 54 + r8 : 32 + r2) * 544;
        xa[2] = st + (hv ? 57 + r8 : 41 + r4) * 544; xa[3] = st + (hv ? 60 + r8 : 46 + r4) * 544;
#pragma unroll
        for (int i = 0; i < 4; ++i) kt[i] = (f32x4){0.f, 0.f, 0.f, 0.f};
#pragma unroll
        for (int ks = 0; ks < 8; ++ks) {
          const v4u Y = lds16(st + s16 * 544 + ks * 64);
#pragma unroll
          for (int i = 0; i < 4; ++i) kt[i] = mfma_h(lds16(xa[i] + ks * 64), Y, kt[i]);
        }
      }
      lds_barrier();
      if (rd == 1) stage_wo(g_wo, cg, lw1k, w, lane);
      if (rd == 0) STAMP(7);
      if (act) {
        const unsigned kb = (unsigned)(size_t)(lds + cs * LA_CH) + 16 + 2 * (2032 - 6 - 1024 * hv - 16 * s16 + 4 * g);
#pragma unroll
        for (int i = 0; i < 4; ++i) {
          f32x4 v = kt[i] * kscale;
          if (i == 0 && hv == 0 && lane == 48) v[3] += dskip;
          const unsigned d0 = pk_bf16(v[0], v[1]), d1 = pk_bf16(v[2], v[3]);
#pragma unroll
          for (int k = 0; k < 4; ++k) { const int kap = (k + (s16 >> 2)) & 3; ds_write2(kb + kap * (LA_KC + 4) - 512 * i, d0, d1); }
        }
        if (hv == 0) { const int k = g; if (s16 < 15 - k) *(LAS unsigned*)(lds + cs * LA_CH + k * LA_KC + 16 + 2 * (2042 + 2 * k) + s16 * 4) = 0u; }
        else if (lane < 56) *(LAS v4u*)(lds + cs * LA_CH + LA_U0 + (lane >= 28 ? LA_UC - 448 : 0) + lane * 16) = (v4u){0u, 0u, 0u, 0u};
      }
      if (rd == 0) asm volatile("s_waitcnt vmcnt(0)" : "+v"(ur[0]), "+v"(ur[1]), "+v"(ur[2]), "+v"(ur[3]), "+v"(uh), "+v"(pf[0]), "+v"(pf[1]) :: "memory");
#pragma unroll
      for (int c = 0; c < 4; ++c) {
        if (c < ns) {
          const int e = ush + (rd ? 4 : c), dw = e >> 1;
          const unsigned sel = (e & 1) ? 0x07060302u : 0x05040100u;
          auto pick = [&](const v3u& v) { return dw == 0 ? v.x : (dw == 1 ? v.y : v.z); };
          const unsigned p01 = __builtin_amdgcn_perm(pick(ur[1]), pick(ur[0]), sel), p23 = __builtin_amdgcn_perm(pick(ur[3]), pick(ur[2]), sel);
          const unsigned hq = hr < 0 ? 0u : __builtin_amdgcn_perm(pick(uh), 0u, sel);
          const unsigned q23 = __builtin_amdgcn_update_dpp(hq, p23, 0x138, 0xf, 0xf, false);
          const lptr up = lds + c * LA_CH + LA_U0 + 448 + rt * 8;
          *(LAS v2u*)(up) = (v2u){__builtin_amdgcn_alignbit(p01, q23, 16), __builtin_amdgcn_alignbit(p23, p01, 16)};
          *(LAS v2u*)(up + LA_UC) = (v2u){p01, p23};
        }
      }
      lds_barrier();
      if (rd == 0) STAMP(2);
      f32x4 y[8];
      if (act) {
#pragma unroll
        for (int t = 0; t < 8; ++t) y[t] = (f32x4){0.f, 0.f, 0.f, 0.f};
        const lptr kp = lds + cs * LA_CH + tp * LA_KC + 16 + 2 * (2040 - 1792 - 128 * hv + 8 * (g - tq));
        const lptr up = lds + cs * LA_CH + LA_U0 + (s16 & 1) * LA_UC + 2 * (224 - 128 * hv + 32 * (s16 >> 1) + 8 * g);
        v4u Kf[2][8], Uf[2][8];
#pragma unroll
        for (int d = 0; d < 8; ++d) { Kf[0][d] = lds16(kp + 512 * (7 - d)); Uf[0][d] = lds16(up + 512 * d); }
#pragma unroll
        for (int sg = 0; sg < 4; ++sg) {
          SCHED();
          if (sg < 3) {
#pragma unroll
            for (int d = 0; d < 8; ++d) { Kf[(sg + 1) & 1][d] = lds16(kp - 64 * (sg + 1) + 512 * (7 - d)); Uf[(sg + 1) & 1][d] = lds16(up - 64 * (sg + 1) + 512 * d); }
          }
          SCHED();
#pragma unroll
          for (int d = 0; d < 8; ++d)
#pragma unroll
            for (int t = d; t < 8; ++t) y[t] = mfma_bf(Uf[sg & 1][t - d], Kf[sg & 1][d], y[t]);
        }
        SCHED();
#pragma unroll
        for (int i = 0; i < 4; ++i) *(LAS f32x4*)(lds + (rd ? LA_STG : LA_RED) + w * 4096 + i * 1024 + lane * 16) = sel4(hv, y[i], y[4 + i]);
      }
      lds_barrier();
      if (rd == 0) STAMP(3);
      if (act) {
        const unsigned ob = (unsigned)(size_t)(lds + LA_OUT + cs * 4128) + 4 * (512 * hv + 32 * g + s16);
#pragma unroll
        for (int i = 0; i < 4; ++i) {
          const f32x4 v = sel4(hv, y[4 + i], y[i]) + *(LAS f32x4*)(lds + (rd ? LA_STG : LA_RED) + (w ^ 1) * 4096 + i * 1024 + lane * 16);
          asm volatile("ds_write2_b32 %0, %1, %2 offset1:16" :: "v"(ob + 512 * i), "v"(pk_bf16(v[0], v[1])), "v"(pk_bf16(v[2], v[3])) : "memory");
        }
      }
      lds_barrier();
      if (rd == 0 && nch == 4) stage_wo(g_wo, cg, lw1k, w, lane);
      {
        char* op = g_out + (size_t)(tid * 4) * 2304 + 2 * (c0 + (rd ? 4 : 0));
        const lptr ob = lds + LA_OUT + tid * 8;
        if (rd == 0) {
          const v2u a = *(LAS v2u*)(ob), b = *(LAS v2u*)(ob + 4128), c = *(LAS v2u*)(ob + 2 * 4128), d = *(LAS v2u*)(ob + 3 * 4128);
          const v2u r0 = {__builtin_amdgcn_perm(b.x, a.x, 0x05040100u), __builtin_amdgcn_perm(d.x, c.x, 0x05040100u)};
          const v2u r1 = {__builtin_amdgcn_perm(b.x, a.x, 0x07060302u), __builtin_amdgcn_perm(d.x, c.x, 0x07060302u)};
          const v2u r2 = {__builtin_amdgcn_perm(b.y, a.y, 0x05040100u), __builtin_amdgcn_perm(d.y, c.y, 0x05040100u)};
          const v2u r3 = {__builtin_amdgcn_perm(b.y, a.y, 0x07060302u), __builtin_amdgcn_perm(d.y, c.y, 0x07060302u)};
          asm volatile("global_store_dwordx2 %0, %1, off\n\tglobal_store_dwordx2 %0, %2, off offset:2304" :: "v"(op), "v"(r0), "v"(r1) : "memory");
          asm volatile("global_store_dwordx2 %0, %1, off\n\tglobal_store_dwordx2 %0, %2, off offset:2304" :: "v"(op + 4608), "v"(r2), "v"(r3) : "memory");
        } else {
          const v2u a = *(LAS v2u*)(ob);
          *(u16*)(op) = (u16)a.x; *(u16*)(op + 2304) = (u16)(a.x >> 16); *(u16*)(op + 4608) = (u16)a.y; *(u16*)(op + 6912) = (u16)(a.y >> 16);
        }
      }
      if (rd == 0) STAMP(4);
    };
    round(IC(0));
    if (nch == 5) round(IC(1));
    STAMP(5);
    DRAIN(); BAR(); asm volatile("" ::: "memory");
    STAMP(8);
  }

  {
    const int m = w & 3, kh = w >> 2, mt = xcd * 16 + (bi >> 3) * 4 + m;
    v4u wf[9][5];
    {
      const lptr wb = lds + L_WO + (288 * kh + 4 * g + tq) * 144 + tp * 8;
      const lptr ws = lds + L_WO + (288 * kh + 72 * g + 16 + tq) * 144 + tp * 8;
#pragma unroll
      for (int i = 0; i < 8; ++i)
#pragma unroll
        for (int n = 0; n < 5; ++n) wf[i][n] = trd2(wb + i * 36 * 144 + n * 32, wb + (i * 36 + 20) * 144 + n * 32);
#pragma unroll
      for (int n = 0; n < 5; ++n) wf[8][n] = trd2(ws + n * 32, ws + 36 * 144 + n * 32);
    }
    WAITCNT(63, 0);
    STAMP(9);
    if (w == 0) poll_flags(g_ws + WS_F2 + xcd * 128, 16, tag, lane);
    BAR(); asm volatile("" ::: "memory");
    STAMP(10);
    v4u of[9];
#pragma unroll
    for (int i = 0; i < 8; ++i) of[i] = __builtin_amdgcn_raw_buffer_load_b128(ws_rs, WS_OM + (mt * 16 + 8 * kh) * 1024 + lane * 16, i * 1024, 16);
    {
      const unsigned so = WS_OS + ((xcd * 16 + 8 * kh + 2 * g) * 256 + (mt & 15) * 16 + s16) * 8;
      const v2u a = __builtin_amdgcn_raw_buffer_load_b64(ws_rs, so, 0, 16), b = __builtin_amdgcn_raw_buffer_load_b64(ws_rs, so, 2048, 16);
      of[8] = (v4u){a.x, a.y, b.x, b.y};
    }
    f32x4 oc[5];
#pragma unroll
    for (int n = 0; n < 5; ++n) oc[n] = (f32x4){0.f, 0.f, 0.f, 0.f};
#pragma unroll
    for (int i = 0; i < 9; ++i)
#pragma unroll
      for (int n = 0; n < 5; ++n) oc[n] = mfma_bf(wf[i][n], of[i], oc[n]);
    if (kh) {
#pragma unroll
      for (int n = 0; n < 5; ++n) *(LAS f32x4*)(lds + L_RED + m * 5120 + n * 1024 + lane * 16) = oc[n];
    }
    lds_barrier();
    if (!kh) {
      const lptr sb = lds + L_OST + m * 16 * 176;
#pragma unroll
      for (int n = 0; n < 5; ++n) {
        const f32x4 v = oc[n] + *(LAS f32x4*)(lds + L_RED + m * 5120 + n * 1024 + lane * 16);
        *(LAS v2u*)(sb + s16 * 176 + n * 32 + g * 8) = (v2u){pk_bf16(v[0], v[1]), pk_bf16(v[2], v[3])};
      }
      asm volatile("" ::: "memory");
      char* ob = g_out + (size_t)mt * 16 * 2304 + 1152 + cg * 144;
#pragma unroll
      for (int j = 0; j < 3; ++j) {
        const unsigned c = j * 64 + lane, r = (c * 7282u) >> 16, k = c - r * 9u;
        if (c < 144) *(v4u*)(ob + r * 2304u + k * 16u) = lds16(sb + r * 176 + k * 16);
      }
    }
    if (blk == 0 && w == 4) {
#pragma unroll
      for (int j = 0; j < 4; ++j) poll_flags(g_ws + WS_F0 + j * 512, 64, tag, lane);
      if (lane == 0) flag_store(g_ws + WS_EPOCH, tag);
    }
  }
  if (w == 7 && blk < 8) { while (__builtin_amdgcn_s_memrealtime() - pad_t0 < 3000) __builtin_amdgcn_s_sleep(4);
    __builtin_amdgcn_fence(__ATOMIC_RELEASE, ""); DRAIN();
    while (__builtin_amdgcn_s_memrealtime() - pad_t0 < 4000) __builtin_amdgcn_s_sleep(4); }
}

extern "C" void kernel_launch(void* const* d_in, const int* in_sizes, int n_in, void* d_out, int out_size, void* d_ws, size_t ws_size,
                              hipStream_t stream) {
  (void)in_sizes; (void)n_in; (void)out_size; (void)ws_size;
  (void)hipFuncSetAttribute((const void*)fused_kernel, hipFuncAttributeMaxDynamicSharedMemorySize, LDS_BYTES);
  fused_kernel<<<256, 512, LDS_BYTES, stream>>>((const char*)d_in[0], (const char*)d_in[1], (const char*)d_in[2], (const char*)d_in[3],
                                                (const char*)d_in[4], (const char*)d_in[5], (const char*)d_in[6], (const char*)d_in[7],
                                                (const char*)d_in[8], (const char*)d_in[9], (const char*)d_in[10], (char*)d_out, (char*)d_ws);
}
```

```cpp
#include <hip/hip_runtime.h>
#include <type_traits>

typedef unsigned short u16;
typedef unsigned long long u64;
typedef unsigned v4u __attribute__((ext_vector_type(4)));
typedef unsigned v3u __attribute__((ext_vector_type(3)));
typedef unsigned v2u __attribute__((ext_vector_type(2)));
typedef float f32x4 __attribute__((ext_vector_type(4)));
typedef float f32x2 __attribute__((ext_vector_type(2)));
typedef short s16x4 __attribute__((ext_vector_type(4)));
typedef __bf16 bf16x8 __attribute__((ext_vector_type(8)));
typedef __bf16 bf16x2 __attribute__((ext_vector_type(2)));
typedef _Float16 f16x8 __attribute__((ext_vector_type(8)));
typedef _Float16 f16x2 __attribute__((ext_vector_type(2)));
#define LAS __attribute__((address_space(3)))
#define GAS __attribute__((address_space(1)))
#define DI __device__ __forceinline__
typedef LAS char* lptr;

constexpr int WS_EPOCH = 0;
constexpr int WS_F0 = 256;
constexpr int WS_F1 = 4096;
constexpr int WS_F2 = 8192;
constexpr int WS_PART = 16384;
constexpr int PART_B = 5632;
constexpr int WS_OM = 1 << 20;
constexpr int WS_OS = 3 << 20;
constexpr int WS_DBG = 4 << 20;
constexpr int WS_USED = 5 << 20;
constexpr int LDS_BYTES = 160 * 1024;

constexpr int LB_W = 0, LB_WSLOT = 24576, LB_X = 73728, LB_WPHI = 141312;
constexpr int LB_IMG = 0, L_WO = 57344, LB_KVF = 143360;
constexpr int L_RED = 0, L_OST = 20480;
constexpr int LA_STG = 34272;
constexpr int LA_KC = 4160, LA_UC = 4640, LA_U0 = 4 * LA_KC, LA_CH = 4 * LA_KC + 2 * LA_UC;
constexpr int LA_RED = 103680;
constexpr int LA_OUT = 140544;

#define PROFW 0
#define STAMP(i) do {} while (0)

DI float bf2f(unsigned lo16) { return __uint_as_float(lo16 << 16); }
DI unsigned pk_bf16(float a, float b) { f32x2 v = {a, b}; return __builtin_bit_cast(unsigned, __builtin_convertvector(v, bf16x2)); }
DI unsigned pk_f16(float a, float b) { f32x2 v = {a, b}; return __builtin_bit_cast(unsigned, __builtin_convertvector(v, f16x2)); }
DI float rnd_bf16(float a) { return bf2f(pk_bf16(a, a) & 0xffffu); }
DI f32x4 mfma_bf(v4u a, v4u b, f32x4 c) { return __builtin_amdgcn_mfma_f32_16x16x32_bf16(__builtin_bit_cast(bf16x8, a), __builtin_bit_cast(bf16x8, b), c, 0, 0, 0); }
DI f32x4 mfma_h(v4u a, v4u b, f32x4 c) { return __builtin_amdgcn_mfma_f32_16x16x32_f16(__builtin_bit_cast(f16x8, a), __builtin_bit_cast(f16x8, b), c, 0, 0, 0); }
DI v2u trd(lptr p) { return __builtin_bit_cast(v2u, __builtin_amdgcn_ds_read_tr16_b64_v4i16((LAS s16x4*)p)); }
DI v4u trd2(lptr p0, lptr p1) { v2u a = trd(p0), b = trd(p1); return (v4u){a.x, a.y, b.x, b.y}; }
DI v4u lds16(lptr p) { return *(LAS v4u*)p; }
template <int IMM> DI void glds(const char* base, unsigned voff, unsigned lbase) {
  asm volatile("s_add_u32 m0, %2, %3\n\ts_nop 0\n\tglobal_load_lds_dwordx4 %0, %1" :: "v"(voff), "s"(base), "s"(lbase), "i"(IMM) : "memory", "scc");
}
#define IC(n) std::integral_constant<int, (n)>{}
template <int N, int I = 0, class F> DI void static_for(F&& f) { if constexpr (I < N) { f(std::integral_constant<int, I>{}); static_for<N, I + 1>(f); } }
DI void gld32(unsigned& d, const char* base, unsigned voff) { asm volatile("global_load_dword %0, %1, %2" : "=v"(d) : "v"(voff), "s"(base) : "memory"); }
DI void gld16(unsigned& d, const char* base, unsigned voff) { asm volatile("global_load_ushort %0, %1, %2" : "=v"(d) : "v"(voff), "s"(base) : "memory"); }
DI void gld96(v3u& d, const char* base, unsigned voff) { asm volatile("global_load_dwordx3 %0, %1, %2" : "=v"(d) : "v"(voff), "s"(base) : "memory"); }
#define WAITCNT(vm, lgkm) __builtin_amdgcn_s_waitcnt(((vm) & 15) | (((vm) >> 4) << 14) | 0x70 | ((lgkm) << 8))
#define BAR() __builtin_amdgcn_s_barrier()
#define SCHED() __builtin_amdgcn_sched_barrier(0)
#define DRAIN() asm volatile("s_waitcnt vmcnt(0)" ::: "memory")
DI void lds_barrier() { asm volatile("" ::: "memory"); WAITCNT(63, 0); BAR(); asm volatile("" ::: "memory"); }
DI f32x4 sel4(bool c, f32x4 a, f32x4 b) { return (f32x4){c ? a[0] : b[0], c ? a[1] : b[1], c ? a[2] : b[2], c ? a[3] : b[3]}; }
DI void ds_write2(unsigned addr, unsigned a, unsigned b) { asm volatile("ds_write2_b32 %0, %1, %2 offset1:1" :: "v"(addr), "v"(a), "v"(b) : "memory"); }

DI u64 mix64(u64 z) { z += 0x9E3779B97F4A7C15ull; z = (z ^ (z >> 30)) * 0xBF58476D1CE4E5B9ull; z = (z ^ (z >> 27)) * 0x94D049BB133111EBull; return z ^ (z >> 31); }
DI u64 read_tag(const char* ws) {
  u64 e; asm volatile("s_load_dwordx2 %0, %1, 0x0\n\ts_waitcnt lgkmcnt(0)" : "=s"(e) : "s"(ws) : "memory");
  return mix64(e);
}
DI void flag_store(char* p, u64 tag) { __hip_atomic_store((GAS u64*)p, tag, __ATOMIC_RELAXED, __HIP_MEMORY_SCOPE_AGENT); }
DI void poll_flags(char* p, int n, u64 tag, int lane) {
  GAS u64* f = (GAS u64*)p + (lane < n ? lane : 0);
  for (unsigned spins = 0; spins < (1u << 22); ++spins) {
    u64 v = __hip_atomic_load(f, __ATOMIC_RELAXED, __HIP_MEMORY_SCOPE_AGENT);
    if (__all(v == tag)) break;
    __builtin_amdgcn_s_sleep(1);
  }
  __builtin_amdgcn_fence(__ATOMIC_ACQUIRE, "wavefront");
}

DI void swap32(float& a, float& b) { auto r = __builtin_amdgcn_permlane32_swap(__float_as_uint(a), __float_as_uint(b), false, false); a = __uint_as_float(r[0]); b = __uint_as_float(r[1]); }
DI void swap16(float& a, float& b) { auto r = __builtin_amdgcn_permlane16_swap(__float_as_uint(a), __float_as_uint(b), false, false); a = __uint_as_float(r[0]); b = __uint_as_float(r[1]); }

DI void features(const f32x4& F0, const f32x4& F1, float e, v4u& P0, v4u& P1, unsigned& PX) {
  const float L = 1.4426950408889634f;
  float a = fmaxf(fmaxf(F0[0], F0[1]), fmaxf(F0[2], F0[3])), b = fmaxf(fmaxf(F1[0], F1[1]), fmaxf(F1[2], F1[3]));
  float c = -fminf(fminf(F0[0], F0[1]), fminf(F0[2], F0[3])), d = -fminf(fminf(F1[0], F1[1]), fminf(F1[2], F1[3]));
  swap32(a, b); swap32(c, d);
  float r1 = fmaxf(fmaxf(a, b), e), r2 = fmaxf(fmaxf(c, d), -e);
  swap16(r1, r2);
  float t1 = fmaxf(r1, r2) * -L, t2 = t1;
  swap16(t1, t2);
  float m0 = t1, m1 = t1, n0 = t2, n1 = t2;
  swap32(m0, m1); swap32(n0, n1);
  float p0[4], q0[4], p1[4], q1[4];
#pragma unroll
  for (int j = 0; j < 4; ++j) {
    p0[j] = __builtin_amdgcn_exp2f(fmaf(F0[j], L, m0)); q0[j] = __builtin_amdgcn_exp2f(fmaf(F0[j], -L, n0));
    p1[j] = __builtin_amdgcn_exp2f(fmaf(F1[j], L, m1)); q1[j] = __builtin_amdgcn_exp2f(fmaf(F1[j], -L, n1));
  }
  float px = __builtin_amdgcn_exp2f(fmaf(e, L, t1)), qx = __builtin_amdgcn_exp2f(fmaf(e, -L, t2));
  a = (p0[0] + p0[1]) + (p0[2] + p0[3]); b = (p1[0] + p1[1]) + (p1[2] + p1[3]);
  c = (q0[0] + q0[1]) + (q0[2] + q0[3]); d = (q1[0] + q1[1]) + (q1[2] + q1[3]);
  swap32(a, b); swap32(c, d);
  r1 = a + b + px; r2 = c + d + qx;
  swap16(r1, r2);
  t1 = __builtin_amdgcn_rcpf(r1 + r2); t2 = t1;
  swap16(t1, t2);
  m0 = t1; m1 = t1; n0 = t2; n1 = t2;
  swap32(m0, m1); swap32(n0, n1);
  P0 = (v4u){pk_bf16(p0[0] * m0, p0[1] * m0), pk_bf16(p0[2] * m0, p0[3] * m0), pk_bf16(q0[0] * n0, q0[1] * n0), pk_bf16(q0[2] * n0, q0[3] * n0)};
  P1 = (v4u){pk_bf16(p1[0] * m1, p1[1] * m1), pk_bf16(p1[2] * m1, p1[3] * m1), pk_bf16(q1[0] * n1, q1[1] * n1), pk_bf16(q1[2] * n1, q1[3] * n1)};
  PX = pk_bf16(px * t1, qx * t2);
}

struct cplx2 { f32x2 r, i; };
DI cplx2 cmul(const cplx2& a, const cplx2& b) { cplx2 c; c.r = a.r * b.r - a.i * b.i; c.i = a.r * b.i + a.i * b.r; return c; }
DI cplx2 cexp2(f32x2 al, f32x2 wr, float p) {
  cplx2 c;
#pragma unroll
  for (int k = 0; k < 2; ++k) {
    float m = __builtin_amdgcn_exp2f(al[k] * p), f = __builtin_amdgcn_fractf(wr[k] * p);
    c.r[k] = m * __builtin_amdgcn_cosf(f); c.i[k] = m * __builtin_amdgcn_sinf(f);
  }
  return c;
}
DI void put_row(lptr p, const cplx2& v) { *(LAS v2u*)p = (v2u){pk_f16(v.r[0], v.i[0]), pk_f16(v.r[1], v.i[1])}; }

DI void stage_wo(const char* wo, int cg, unsigned lw1k, int w, int lane) {
  int ln = lane; asm volatile("" : "+v"(ln));
  const char* b = wo + cg * 144;
  static_for<11>([&](auto I) {
    constexpr int i = I;
    if (i < 10 || w == 0) {
      const unsigned c = (i * 8 + w) * 64 + ln, r = (c * 7282u) >> 16;
      glds<L_WO + i * 8192>(b, r * 1008u + c * 16u, lw1k);
    }
  });
}

__global__ __launch_bounds__(512, 2) void fused_kernel(
    const char* __restrict__ g_u, const char* __restrict__ g_lar, const char* __restrict__ g_aim, const char* __restrict__ g_cr,
    const char* __restrict__ g_ci, const char* __restrict__ g_ldt, const char* __restrict__ g_dsk, const char* __restrict__ g_x,
    const char* __restrict__ g_w, const char* __restrict__ g_wphi, const char* __restrict__ g_wo, char* __restrict__ g_out, char* g_ws) {
  extern __shared__ __attribute__((aligned(16))) char smem[];
  const lptr lds = (lptr)smem;
  const int tid = threadIdx.x, lane = tid & 63, w = __builtin_amdgcn_readfirstlane(tid >> 6) & 7;
  const int blk = blockIdx.x, xcd = blk & 7, bi = blk >> 3;
  const int g = lane >> 4, s16 = lane & 15, tq = (lane >> 2) & 3, tp = lane & 3;
  const int cg = bi & 7;
  const unsigned lw1k = (unsigned)(size_t)lds + w * 1024;
  STAMP(0);
  asm volatile("" :: "s"(g_u), "s"(g_lar), "s"(g_aim), "s"(g_cr), "s"(g_ci), "s"(g_ldt), "s"(g_dsk), "s"(g_x), "s"(g_w), "s"(g_wphi), "s"(g_wo), "s"(g_out), "s"(g_ws));
  const __amdgpu_buffer_rsrc_t ws_rs = __builtin_amdgcn_make_buffer_rsrc(g_ws, 0, WS_USED, 0x00020000);
  u64 tag;

  if (bi < 16) {
    const int rb = (xcd >> 1) * 2 + (bi >> 3), hp = (xcd & 1) * 8 + (bi & 7);
    const int lr = lane >> 3, lc = lane & 7;
    const int wrow = 8 * w + lr;
    unsigned w_off = wrow * 3456u + (((72 * hp) & ~63) + ((lc ^ ((wrow & 2) | ((wrow >> 1) & 4))) << 4));
    const char* xb0 = g_x + (size_t)(rb * 256 + w * 32) * 1152;
    const char* xb1 = xb0 + 16 * 1152;
    const char *wb1 = g_w + 1152, *wb2 = g_w + 2304;
    unsigned x_off0 = lr * 1152u + ((lc ^ (lane >> 4)) << 4), x_off1 = (8 + lr) * 1152u + ((lc ^ (4 + (lane >> 4))) << 4);
    const unsigned lw8k = (unsigned)(size_t)lds + w * 8192;
    auto issue_w = [&](auto T) {
      constexpr int o = LB_W + (T % 3) * LB_WSLOT;
      glds<o>(g_w, w_off, lw1k); glds<o + 8192>(wb1, w_off, lw1k); glds<o + 16384>(wb2, w_off, lw1k);
      w_off += 64 * 3456; asm volatile("" : "+v"(w_off));
    };
    auto issue_x = [&](auto T) {
      constexpr int o = LB_X + (T & 1) * 4096;
      glds<o>(xb0, x_off0, lw8k); glds<o + 1024>(xb0, x_off1, lw8k); glds<o + 2048>(xb1, x_off0, lw8k); glds<o + 3072>(xb1, x_off1, lw8k);
      x_off0 += 128; x_off1 += 128; asm volatile("" : "+v"(x_off0), "+v"(x_off1));
    };
    issue_w(IC(0)); issue_x(IC(0));
    if (w < 2) { const int c = w * 64 + lane; glds<LB_WPHI>(g_wphi + hp * 1296, (c < 81 ? c : 80) * 16, lw1k); }
    issue_w(IC(1)); issue_x(IC(1)); issue_w(IC(2));
    tag = read_tag(g_ws);
    const int ub = ((72 * hp) & 63) >> 3, fsw = (tq & 2) | ((g & 1) << 2), trow = (8 * g + tq) * 128;
    const lptr wa0 = lds + trow + ((((ub + tp) >> 1) ^ fsw) << 4) + ((ub + tp) & 1) * 8;
    const lptr wa1 = lds + trow + ((((ub + 5 + tp) >> 1) ^ fsw) << 4) + ((ub + 5 + tp) & 1) * 8;
    const lptr was = lds + trow + ((((ub + 4) >> 1) ^ fsw) << 4) + ((ub + 4) & 1) * 8 + (tp < 2 ? tp : 2) * 8192;
    const lptr xa0 = lds + LB_X + w * 8192 + s16 * 128 + ((g ^ (s16 >> 1)) << 4);
    const lptr xa1 = lds + LB_X + w * 8192 + s16 * 128 + (((4 + g) ^ (s16 >> 1)) << 4);
    auto read_w = [&](v4u (&f)[7], int t, int ks) {
      const int o = (t % 3) * LB_WSLOT + ks * 4096;
#pragma unroll
      for (int p = 0; p < 3; ++p) {
        f[2 * p] = trd2(wa0 + o + p * 8192, wa0 + o + p * 8192 + 512);
        f[2 * p + 1] = trd2(wa1 + o + p * 8192, wa1 + o + p * 8192 + 512);
      }
      f[6] = trd2(was + o, was + o + 512);
    };
    auto read_x = [&](v4u (&f)[4], int t) {
      const int o = (t & 1) * 4096;
      f[0] = lds16(xa0 + o); f[1] = lds16(xa0 + o + 2048); f[2] = lds16(xa1 + o); f[3] = lds16(xa1 + o + 2048);
    };
    f32x4 acc[7][2];
#pragma unroll
    for (int n = 0; n < 7; ++n) { acc[n][0] = (f32x4){0.f, 0.f, 0.f, 0.f}; acc[n][1] = acc[n][0]; }
    v4u wf0[7], wf1[7], xc[4], xn[4];
    unsigned wm[4][2], wsp[4];
    WAITCNT(10, 15); BAR(); asm volatile("" ::: "memory");
    STAMP(1);
    if (tid == 0) flag_store(g_ws + WS_F0 + blk * 8, tag);
    read_x(xc, 0); read_w(wf0, 0, 0);
    WAITCNT(63, 0); asm volatile("" ::: "memory");
    issue_x(IC(2));
    static_for<9>([&](auto T) {
      constexpr int t = T;
      SCHED();
      read_w(wf1, t, 1);
      if (t == 2) {
        const lptr wp = lds + LB_WPHI;
#pragma unroll
        for (int fi = 0; fi < 4; ++fi) {
          const int hh = fi & 1, f = fi < 2 ? s16 : 16 + ((s16 >> 2) & 1);
          const lptr b = wp + hh * 648 + f * 2;
          const int d0 = 4 * g + 2 * hh, ds = hh ? 0 : 16;
          unsigned e0 = *(LAS u16*)(b + d0 * 36), e1 = *(LAS u16*)(b + d0 * 36 + 36), e2 = *(LAS u16*)(b + d0 * 36 + 72), e3 = *(LAS u16*)(b + d0 * 36 + 108);
          unsigned e4 = *(LAS u16*)(b + ds * 36), e5 = *(LAS u16*)(b + ds * 36 + 36);
          const bool z = fi >= 2 && (s16 >> 3) != hh;
          wm[fi][0] = z ? 0u : (e0 | (e1 << 16)); wm[fi][1] = z ? 0u : (e2 | (e3 << 16)); wsp[fi] = z ? 0u : (e4 | (e5 << 16));
        }
      }
      SCHED();
#pragma unroll
      for (int n = 0; n < 7; ++n) { acc[n][0] = mfma_bf(wf0[n], xc[0], acc[n][0]); acc[n][1] = mfma_bf(wf0[n], xc[1], acc[n][1]); }
      SCHED();
      if (t < 7) WAITCNT(7, 0); else WAITCNT(0, 0);
      BAR(); asm volatile("" ::: "memory");
      if constexpr (t < 6) issue_w(IC(t + 3));
      if (t == 8) stage_wo(g_wo, cg, lw1k, w, lane);
      SCHED();
      if (t < 8) { read_x(xn, t + 1); read_w(wf0, t + 1, 0); }
      SCHED();
#pragma unroll
      for (int n = 0; n < 7; ++n) { acc[n][0] = mfma_bf(wf1[n], xc[2], acc[n][0]); acc[n][1] = mfma_bf(wf1[n], xc[3], acc[n][1]); }
      SCHED();
      if (t < 8) {
        WAITCNT(63, 0); asm volatile("" ::: "memory");
        if constexpr (t < 6) issue_x(IC(t + 3));
#pragma unroll
        for (int i = 0; i < 4; ++i) xc[i] = xn[i];
      }
    });
    STAMP(2);
    auto phi = [&](int part, int m, v4u& P0, v4u& P1, unsigned& PX) {
      const f32x4 &T0 = acc[2 * part][m], &T1 = acc[2 * part + 1][m], &S = acc[6][m];
      const unsigned s0 = pk_bf16(S[0], S[1]), s1 = pk_bf16(S[2], S[3]);
      const v4u B0 = {pk_bf16(T0[0], T0[1]), pk_bf16(T0[2], T0[3]), s0, s1}, B1 = {pk_bf16(T1[0], T1[1]), pk_bf16(T1[2], T1[3]), s0, s1};
      const bool on = g == part;
      const f32x4 z = {0.f, 0.f, 0.f, 0.f};
      const f32x4 F0 = mfma_bf((v4u){wm[0][0], wm[0][1], on ? wsp[0] : 0u, 0u}, B0, z);
      const f32x4 F1 = mfma_bf((v4u){wm[1][0], wm[1][1], 0u, on ? wsp[1] : 0u}, B1, z);
      f32x4 F2 = mfma_bf((v4u){wm[2][0], wm[2][1], on ? wsp[2] : 0u, 0u}, B0, z);
      F2 = mfma_bf((v4u){wm[3][0], wm[3][1], 0u, on ? wsp[3] : 0u}, B1, F2);
      features(F0, F1, F2[0], P0, P1, PX);
    };
    {
      const int sw = s16 >> 2;
#pragma unroll
      for (int m = 0; m < 2; ++m) {
        v4u P0, P1; unsigned PX;
        phi(1, m, P0, P1, PX);
        const lptr row = lds + LB_IMG + (32 * w + 16 * m + s16) * 224, rp = row + ((g ^ sw) << 3);
        *(LAS v2u*)(rp) = (v2u){P0.x, P0.y}; *(LAS v2u*)(rp + 32) = (v2u){P0.z, P0.w};
        *(LAS v2u*)(rp + 64) = (v2u){P1.x, P1.y}; *(LAS v2u*)(rp + 96) = (v2u){P1.z, P1.w};
        *(LAS unsigned*)(row + 128 + (((g >> 1) ^ sw) << 3) + (g & 1) * 4) = PX;
        const f32x4 &V0 = acc[4][m], &V1 = acc[5][m], &S = acc[6][m];
        *(LAS v2u*)(rp + 160) = (v2u){pk_bf16(V0[0], V0[1]), pk_bf16(V0[2], V0[3])};
        *(LAS v2u*)(rp + 192) = (v2u){pk_bf16(V1[0], V1[1]), pk_bf16(V1[2], V1[3])};
        if (g >= 2) *(LAS v2u*)(rp + 128) = g == 2 ? (v2u){pk_bf16(S[0], S[1]), pk_bf16(S[2], S[3])} : (v2u){0x3f803f80u, 0x3f803f80u};
      }
    }
    lds_barrier();
    STAMP(3);
    {
      const int a = w & 3, h = a >> 1;
      const lptr ib = lds + LB_IMG + (4 * g + tq) * 224 + ((tp ^ g) << 3);
      const lptr pa = ib + a * 32, pb = ib + (w < 4 ? 160 + 32 * h : 128), pe = ib + 128;
      const bool ex = (w & 5) == 0 || w == 4;
      f32x4 c0 = {0.f, 0.f, 0.f, 0.f}, c1 = c0;
#pragma unroll
      for (int ks = 0; ks < 8; ++ks) {
        const int o = ks * 32 * 224;
        const v4u A = trd2(pa + o, pa + o + 16 * 224), B = trd2(pb + o, pb + o + 16 * 224);
        c0 = mfma_bf(A, B, c0);
        if (ex) c1 = mfma_bf(trd2(pe + o, pe + o + 16 * 224), B, c1);
      }
      const unsigned pbase = WS_PART + (hp * 8 + rb) * PART_B;
      if (w < 4) {
        __builtin_amdgcn_raw_buffer_store_b128(__builtin_bit_cast(v4u, c0), ws_rs, pbase + (a * 64 + lane) * 16, 0, 16);
        if (ex && g == h) __builtin_amdgcn_raw_buffer_store_b128(__builtin_bit_cast(v4u, c1), ws_rs, pbase + (304 + h * 16 + s16) * 16, 0, 16);
      } else {
        const int rec = s16 == 12 ? 0 : s16 - 7 - 2 * h;
        if (s16 == 12 || rec == 1 || rec == 2) __builtin_amdgcn_raw_buffer_store_b128(__builtin_bit_cast(v4u, c0), ws_rs, pbase + (256 + (a * 3 + rec) * 4 + g) * 16, 0, 16);
        const int rx = s16 == 12 ? 0 : s16 - 7 - 2 * g;
        if (ex && g < 2 && (s16 == 12 || rx == 1 || rx == 2)) __builtin_amdgcn_raw_buffer_store_b128(__builtin_bit_cast(v4u, c1), ws_rs, pbase + (336 + g * 3 + rx) * 16, 0, 16);
      }
    }
    DRAIN(); BAR(); asm volatile("" ::: "memory");
    if (tid == 0) flag_store(g_ws + WS_F1 + hp * 128 + rb * 8, tag);
    STAMP(4);
    v4u Q0[2], Q1[2]; unsigned QX[2];
    phi(0, 0, Q0[0], Q1[0], QX[0]); phi(0, 1, Q0[1], Q1[1], QX[1]);
    STAMP(5);
    if (w == 0) poll_flags(g_ws + WS_F1 + hp * 128, 8, tag, lane);
    BAR(); asm volatile("" ::: "memory");
    STAMP(6);
    if (tid < 342) {
      v4u pv[8];
#pragma unroll
      for (int r = 0; r < 8; ++r) pv[r] = __builtin_amdgcn_raw_buffer_load_b128(ws_rs, WS_PART + hp * 8 * PART_B + tid * 16, r * PART_B, 16);
      f32x4 sm = __builtin_bit_cast(f32x4, pv[0]);
#pragma unroll
      for (int r = 1; r < 8; ++r) sm += __builtin_bit_cast(f32x4, pv[r]);
      int dst;
      if (tid < 256) dst = (tid >> 7) * 1024 + (tid & 63) * 16 + ((tid >> 6) & 1) * 8;
      else if (tid < 304) { const int i = tid - 256, a = i / 12, rec = (i >> 2) - a * 3; dst = 2048 + (a >> 1) * 256 + rec * 64 + (i & 3) * 16 + (a & 1) * 8; }
      else if (tid < 336) dst = 2560 + (tid - 304) * 8;
      else { const int i = tid - 336; dst = 2816 + (i < 3 ? i : i + 1) * 8; }
      *(LAS v2u*)(lds + LB_KVF + dst) = (v2u){pk_bf16(sm[0], sm[1]), pk_bf16(sm[2], sm[3])};
    }
    lds_barrier();
    STAMP(7);
    {
      const lptr kb = lds + LB_KVF;
      const int rec = tp < 3 ? tp : 0;
      v4u Nm[2], Em[2]; unsigned Nx[2], Ex[2];
#pragma unroll
      for (int h = 0; h < 2; ++h) {
        Nm[h] = lds16(kb + h * 1024 + lane * 16); Em[h] = lds16(kb + 2048 + h * 256 + rec * 64 + g * 16);
        Nx[h] = *(LAS unsigned*)(kb + 2560 + h * 128 + s16 * 8 + (g & 1) * 4); Ex[h] = *(LAS unsigned*)(kb + 2816 + h * 32 + rec * 8 + (g & 1) * 4);
      }
      v2u sp[2];
#pragma unroll
      for (int m = 0; m < 2; ++m) {
        f32x4 O[2], E[2];
#pragma unroll
        for (int h = 0; h < 2; ++h) {
          const v4u Bm = h ? Q1[m] : Q0[m], Bx = {(g >> 1) == h ? QX[m] : 0u, 0u, 0u, 0u};
          const f32x4 z = {0.f, 0.f, 0.f, 0.f};
          O[h] = mfma_bf((v4u){Nx[h], 0u, 0u, 0u}, Bx, mfma_bf(Nm[h], Bm, z));
          E[h] = mfma_bf((v4u){Ex[h], 0u, 0u, 0u}, Bx, mfma_bf(Em[h], Bm, z));
        }
        const float r0 = __builtin_amdgcn_rcpf(E[0][0] + 1e-6f), r1 = __builtin_amdgcn_rcpf(E[1][0] + 1e-6f);
        const v4u ov = {pk_bf16(O[0][0] * r0, O[0][1] * r0), pk_bf16(O[0][2] * r0, O[0][3] * r0), pk_bf16(O[1][0] * r1, O[1][1] * r1), pk_bf16(O[1][2] * r1, O[1][3] * r1)};
        __builtin_amdgcn_raw_buffer_store_b128(ov, ws_rs, WS_OM + ((rb * 16 + 2 * w + m) * 16 + hp) * 1024 + lane * 16, 0, 16);
        sp[m] = (v2u){pk_bf16(E[0][1] * r0, E[0][2] * r0), pk_bf16(E[1][1] * r1, E[1][2] * r1)};
      }
      if (g < 2) __builtin_amdgcn_raw_buffer_store_b64((v2u){g ? sp[1].x : sp[0].x, g ? sp[1].y : sp[0].y}, ws_rs, WS_OS + ((rb * 16 + hp) * 256 + 32 * w + lane) * 8, 0, 16);
    }
    DRAIN(); BAR(); asm volatile("" ::: "memory");
    if (tid == 0) flag_store(g_ws + WS_F2 + rb * 128 + hp * 8, tag);
    STAMP(8);
  } else {
    const int ab = xcd * 16 + (bi - 16);
    const int c0 = 9 * (ab >> 1) + ((ab & 1) ? 5 : 0), nch = (ab & 1) ? 4 : 5;
    const int cs = w >> 1, hv = w & 1;
    unsigned prm[2][4]; unsigned pdt[2], pds[2];
#pragma unroll
    for (int r = 0; r < 2; ++r) {
      const int ch = r ? c0 + nch - 1 : c0 + cs;
      const unsigned o = ch * 256 + lane * 4;
      gld32(prm[r][0], g_lar, o); gld32(prm[r][1], g_aim, o); gld32(prm[r][2], g_cr, o); gld32(prm[r][3], g_ci, o);
      gld16(pdt[r], g_ldt, ch * 2); gld16(pds[r], g_dsk, ch * 2);
    }
    unsigned pf[2];
#pragma unroll
    for (int j = 0; j < 2; ++j) {
      unsigned i = j * 8192 + (bi - 16) * 512 + tid; if (i > 13247) i = 13247;
      const unsigned kt = i / 1472u, r = i - kt * 1472u;
      const unsigned r2 = r - 512u, row = r2 / 15u, q = r2 - row * 15u, part = q / 5u;
      const char* p = r < 512u ? g_x + ((xcd >> 1) * 512 + r) * 1152u + kt * 128u
                               : g_w + (kt * 64u + row) * 3456u + part * 1152u + ((xcd & 1) * 4 + q - part * 5u) * 128u;
      asm volatile("global_load_dword %0, %1, off" : "=v"(pf[j]) : "v"(p) : "memory");
    }
    int ua = (2 * c0) & ~3; if (ua > 1140) ua = 1140;
    const int ush = c0 - (ua >> 1);
    const int rt = (tid + (bi - 16) * 64) & 511;
    v3u ur[4], uh;
#pragma unroll
    for (int j = 0; j < 4; ++j) gld96(ur[j], g_u + ua, (rt * 4 + j) * 1152u);
    const int hr = (rt & ~63) * 4 - 1;
    gld96(uh, g_u + ua, (hr < 0 ? 0 : hr) * 1152u);
    tag = read_tag(g_ws);
    asm volatile("s_waitcnt vmcnt(7)" : "+v"(prm[0][0]), "+v"(prm[0][1]), "+v"(prm[0][2]), "+v"(prm[0][3]), "+v"(prm[1][0]), "+v"(prm[1][1]), "+v"(prm[1][2]),
                 "+v"(prm[1][3]), "+v"(pdt[0]), "+v"(pdt[1]), "+v"(pds[0]), "+v"(pds[1]) :: "memory");
    STAMP(6);
    if (tid == 0) flag_store(g_ws + WS_F0 + blk * 8, tag);

    auto round = [&](auto RD) {
      constexpr int rd = RD;
      const bool act = rd == 0 || w < 2;
      const int ns = rd == 0 ? 4 : 1;
      float kscale = 0.f, dskip = 0.f;
      if (act) {
        const unsigned q0 = rd ? prm[1][0] : prm[0][0], q1 = rd ? prm[1][1] : prm[0][1], q2 = rd ? prm[1][2] : prm[0][2], q3 = rd ? prm[1][3] : prm[0][3];
        const float dt = rnd_bf16(expf(bf2f(rd ? pdt[1] : pdt[0])));
        dskip = bf2f(rd ? pds[1] : pds[0]);
        f32x2 al, wr; cplx2 C;
        al[0] = -rnd_bf16(expf(bf2f(q0 & 0xffffu))) * dt * 1.4426950408889634f; al[1] = -rnd_bf16(expf(bf2f(q0 >> 16))) * dt * 1.4426950408889634f;
        wr[0] = bf2f(q1 & 0xffffu) * dt * 0.15915494309189535f; wr[1] = bf2f(q1 >> 16) * dt * 0.15915494309189535f;
        C.r[0] = bf2f(q2 & 0xffffu); C.r[1] = bf2f(q2 >> 16); C.i[0] = bf2f(q3 & 0xffffu); C.i[1] = bf2f(q3 >> 16);
        float mx = fmaxf(fmaxf(fabsf(C.r[0]), fabsf(C.r[1])), fmaxf(fabsf(C.i[0]), fabsf(C.i[1])));
#pragma unroll
        for (int o = 32; o >= 1; o >>= 1) mx = fmaxf(mx, __shfl_xor(mx, o));
        int ex = (int)((__float_as_uint(mx) >> 23) & 255u); ex = ex < 30 ? 30 : (ex > 230 ? 230 : ex);
        const float sc = __uint_as_float((unsigned)(267 - ex) << 23);
        kscale = 2.f * dt * __uint_as_float((unsigned)(ex - 28) << 23);
        C.r *= sc; C.i *= sc;
        const lptr st = lds + cs * LA_STG + lane * 8;
        if (hv == 0) {
          const cplx2 e1 = cexp2(al, wr, 1.f); cplx2 e16 = cexp2(al, wr, 16.f); e16.i = -e16.i;
          cplx2 y; y.r = (f32x2){32768.f, 32768.f}; y.i = (f32x2){0.f, 0.f};
          cplx2 x = C;
#pragma unroll
          for (int r = 0; r < 16; ++r) {
            put_row(st + r * 544, y); put_row(st + (16 + r) * 544, x);
            if (r < 15) { y = cmul(y, e16); x = cmul(x, e1); }
          }
        } else {
          const cplx2 e256 = cexp2(al, wr, 256.f), e2 = cexp2(al, wr, 2.f), e4 = cmul(e2, e2), e8 = cmul(e4, e4);
          cplx2 b = C; int row = 32;
#pragma unroll
          for (int t = 1; t < 8; ++t) {
            b = cmul(b, e256);
            const int n = t == 1 ? 9 : (t < 4 ? 5 : 3);
            cplx2 x = b;
#pragma unroll
            for (int r = 0; r < n; ++r) { put_row(st + (row + r) * 544, x); if (r + 1 < n) x = cmul(x, t == 1 ? e2 : (t < 4 ? e4 : e8)); }
            row += n;
          }
        }
      }
      lds_barrier();
      if (rd == 0) STAMP(1);
      f32x4 kt[4];
      if (act) {
        const lptr st = lds + cs * LA_STG + g * 16;
        const int r = 15 - s16;
        const int r2 = (r + ((r >> 1) & 1)) >> 1, r4 = (r + 1 + ((r >> 2) & 1)) >> 2, r8 = (r + 3 + ((r >> 3) & 1)) >> 3;
        lptr xa[4];
        xa[0] = st + (hv ? 51 + r8 : 16 + r) * 544; xa[1] = st + (hv ? 54 + r8 : 32 + r2) * 544;
        xa[2] = st + (hv ? 57 + r8 : 41 + r4) * 544; xa[3] = st + (hv ? 60 + r8 : 46 + r4) * 544;
#pragma unroll
        for (int i = 0; i < 4; ++i) kt[i] = (f32x4){0.f, 0.f, 0.f, 0.f};
#pragma unroll
        for (int ks = 0; ks < 8; ++ks) {
          const v4u Y = lds16(st + s16 * 544 + ks * 64);
#pragma unroll
          for (int i = 0; i < 4; ++i) kt[i] = mfma_h(lds16(xa[i] + ks * 64), Y, kt[i]);
        }
      }
      lds_barrier();
      if (rd == 1) stage_wo(g_wo, cg, lw1k, w, lane);
      if (rd == 0) STAMP(7);
      if (act) {
        const unsigned kb = (unsigned)(size_t)(lds + cs * LA_CH) + 16 + 2 * (2032 - 6 - 1024 * hv - 16 * s16 + 4 * g);
#pragma unroll
        for (int i = 0; i < 4; ++i) {
          f32x4 v = kt[i] * kscale;
          if (i == 0 && hv == 0 && lane == 48) v[3] += dskip;
          const unsigned d0 = pk_bf16(v[0], v[1]), d1 = pk_bf16(v[2], v[3]);
#pragma unroll
          for (int k = 0; k < 4; ++k) { const int kap = (k + (s16 >> 2)) & 3; ds_write2(kb + kap * (LA_KC + 4) - 512 * i, d0, d1); }
        }
        if (hv == 0) { const int k = g; if (s16 < 15 - k) *(LAS unsigned*)(lds + cs * LA_CH + k * LA_KC + 16 + 2 * (2042 + 2 * k) + s16 * 4) = 0u; }
        else if (lane < 56) *(LAS v4u*)(lds + cs * LA_CH + LA_U0 + (lane >= 28 ? LA_UC - 448 : 0) + lane * 16) = (v4u){0u, 0u, 0u, 0u};
      }
      if (rd == 0) asm volatile("s_waitcnt vmcnt(0)" : "+v"(ur[0]), "+v"(ur[1]), "+v"(ur[2]), "+v"(ur[3]), "+v"(uh), "+v"(pf[0]), "+v"(pf[1]) :: "memory");
#pragma unroll
      for (int c = 0; c < 4; ++c) {
        if (c < ns) {
          const int e = ush + (rd ? 4 : c), dw = e >> 1;
          const unsigned sel = (e & 1) ? 0x07060302u : 0x05040100u;
          auto pick = [&](const v3u& v) { return dw == 0 ? v.x : (dw == 1 ? v.y : v.z); };
          const unsigned p01 = __builtin_amdgcn_perm(pick(ur[1]), pick(ur[0]), sel), p23 = __builtin_amdgcn_perm(pick(ur[3]), pick(ur[2]), sel);
          const unsigned hq = hr < 0 ? 0u : __builtin_amdgcn_perm(pick(uh), 0u, sel);
          const unsigned q23 = __builtin_amdgcn_update_dpp(hq, p23, 0x138, 0xf, 0xf, false);
          const lptr up = lds + c * LA_CH + LA_U0 + 448 + rt * 8;
          *(LAS v2u*)(up) = (v2u){__builtin_amdgcn_alignbit(p01, q23, 16), __builtin_amdgcn_alignbit(p23, p01, 16)};
          *(LAS v2u*)(up + LA_UC) = (v2u){p01, p23};
        }
      }
      lds_barrier();
      if (rd == 0) STAMP(2);
      f32x4 y[8];
      if (act) {
#pragma unroll
        for (int t = 0; t < 8; ++t) y[t] = (f32x4){0.f, 0.f, 0.f, 0.f};
        const lptr kp = lds + cs * LA_CH + tp * LA_KC + 16 + 2 * (2040 - 1792 - 128 * hv + 8 * (g - tq));
        const lptr up = lds + cs * LA_CH + LA_U0 + (s16 & 1) * LA_UC + 2 * (224 - 128 * hv + 32 * (s16 >> 1) + 8 * g);
        v4u Kf[2][8], Uf[2][8];
#pragma unroll
        for (int d = 0; d < 8; ++d) { Kf[0][d] = lds16(kp + 512 * (7 - d)); Uf[0][d] = lds16(up + 512 * d); }
#pragma unroll
        for (int sg = 0; sg < 4; ++sg) {
          SCHED();
          if (sg < 3) {
#pragma unroll
            for (int d = 0; d < 8; ++d) { Kf[(sg + 1) & 1][d] = lds16(kp - 64 * (sg + 1) + 512 * (7 - d)); Uf[(sg + 1) & 1][d] = lds16(up - 64 * (sg + 1) + 512 * d); }
          }
          SCHED();
#pragma unroll
          for (int d = 0; d < 8; ++d)
#pragma unroll
            for (int t = d; t < 8; ++t) y[t] = mfma_bf(Uf[sg & 1][t - d], Kf[sg & 1][d], y[t]);
        }
        SCHED();
#pragma unroll
        for (int i = 0; i < 4; ++i) *(LAS f32x4*)(lds + (rd ? LA_STG : LA_RED) + w * 4096 + i * 1024 + lane * 16) = sel4(hv, y[i], y[4 + i]);
      }
      lds_barrier();
      if (rd == 0) STAMP(3);
      if (act) {
        const unsigned ob = (unsigned)(size_t)(lds + LA_OUT + cs * 4128) + 4 * (512 * hv + 32 * g + s16);
#pragma unroll
        for (int i = 0; i < 4; ++i) {
          const f32x4 v = sel4(hv, y[4 + i], y[i]) + *(LAS f32x4*)(lds + (rd ? LA_STG : LA_RED) + (w ^ 1) * 4096 + i * 1024 + lane * 16);
          asm volatile("ds_write2_b32 %0, %1, %2 offset1:16" :: "v"(ob + 512 * i), "v"(pk_bf16(v[0], v[1])), "v"(pk_bf16(v[2], v[3])) : "memory");
        }
      }
      lds_barrier();
      if (rd == 0 && nch == 4) stage_wo(g_wo, cg, lw1k, w, lane);
      {
        char* op = g_out + (size_t)(tid * 4) * 2304 + 2 * (c0 + (rd ? 4 : 0));
        const lptr ob = lds + LA_OUT + tid * 8;
        if (rd == 0) {
          const v2u a = *(LAS v2u*)(ob), b = *(LAS v2u*)(ob + 4128), c = *(LAS v2u*)(ob + 2 * 4128), d = *(LAS v2u*)(ob + 3 * 4128);
          const v2u r0 = {__builtin_amdgcn_perm(b.x, a.x, 0x05040100u), __builtin_amdgcn_perm(d.x, c.x, 0x05040100u)};
          const v2u r1 = {__builtin_amdgcn_perm(b.x, a.x, 0x07060302u), __builtin_amdgcn_perm(d.x, c.x, 0x07060302u)};
          const v2u r2 = {__builtin_amdgcn_perm(b.y, a.y, 0x05040100u), __builtin_amdgcn_perm(d.y, c.y, 0x05040100u)};
          const v2u r3 = {__builtin_amdgcn_perm(b.y, a.y, 0x07060302u), __builtin_amdgcn_perm(d.y, c.y, 0x07060302u)};
          asm volatile("global_store_dwordx2 %0, %1, off\n\tglobal_store_dwordx2 %0, %2, off offset:2304" :: "v"(op), "v"(r0), "v"(r1) : "memory");
          asm volatile("global_store_dwordx2 %0, %1, off\n\tglobal_store_dwordx2 %0, %2, off offset:2304" :: "v"(op + 4608), "v"(r2), "v"(r3) : "memory");
        } else {
          const v2u a = *(LAS v2u*)(ob);
          *(u16*)(op) = (u16)a.x; *(u16*)(op + 2304) = (u16)(a.x >> 16); *(u16*)(op + 4608) = (u16)a.y; *(u16*)(op + 6912) = (u16)(a.y >> 16);
        }
      }
      if (rd == 0) STAMP(4);
    };
    round(IC(0));
    if (nch == 5) round(IC(1));
    STAMP(5);
    DRAIN(); BAR(); asm volatile("" ::: "memory");
    STAMP(8);
  }

  {
    const int m = w & 3, kh = w >> 2, mt = xcd * 16 + (bi >> 3) * 4 + m;
    v4u wf[9][5];
    {
      const lptr wb = lds + L_WO + (288 * kh + 4 * g + tq) * 144 + tp * 8;
      const lptr ws = lds + L_WO + (288 * kh + 72 * g + 16 + tq) * 144 + tp * 8;
#pragma unroll
      for (int i = 0; i < 8; ++i)
#pragma unroll
        for (int n = 0; n < 5; ++n) wf[i][n] = trd2(wb + i * 36 * 144 + n * 32, wb + (i * 36 + 20) * 144 + n * 32);
#pragma unroll
      for (int n = 0; n < 5; ++n) wf[8][n] = trd2(ws + n * 32, ws + 36 * 144 + n * 32);
    }
    WAITCNT(63, 0);
    STAMP(9);
    if (w == 0) poll_flags(g_ws + WS_F2 + xcd * 128, 16, tag, lane);
    BAR(); asm volatile("" ::: "memory");
    STAMP(10);
    v4u of[9];
#pragma unroll
    for (int i = 0; i < 8; ++i) of[i] = __builtin_amdgcn_raw_buffer_load_b128(ws_rs, WS_OM + (mt * 16 + 8 * kh) * 1024 + lane * 16, i * 1024, 16);
    {
      const unsigned so = WS_OS + ((xcd * 16 + 8 * kh + 2 * g) * 256 + (mt & 15) * 16 + s16) * 8;
      const v2u a = __builtin_amdgcn_raw_buffer_load_b64(ws_rs, so, 0, 16), b = __builtin_amdgcn_raw_buffer_load_b64(ws_rs, so, 2048, 16);
      of[8] = (v4u){a.x, a.y, b.x, b.y};
    }
    f32x4 oc[5];
#pragma unroll
    for (int n = 0; n < 5; ++n) oc[n] = (f32x4){0.f, 0.f, 0.f, 0.f};
#pragma unroll
    for (int i = 0; i < 9; ++i)
#pragma unroll
      for (int n = 0; n < 5; ++n) oc[n] = mfma_bf(wf[i][n], of[i], oc[n]);
    if (kh) {
#pragma unroll
      for (int n = 0; n < 5; ++n) *(LAS f32x4*)(lds + L_RED + m * 5120 + n * 1024 + lane * 16) = oc[n];
    }
    lds_barrier();
    if (!kh) {
      const lptr sb = lds + L_OST + m * 16 * 176;
#pragma unroll
      for (int n = 0; n < 5; ++n) {
        const f32x4 v = oc[n] + *(LAS f32x4*)(lds + L_RED + m * 5120 + n * 1024 + lane * 16);
        *(LAS v2u*)(sb + s16 * 176 + n * 32 + g * 8) = (v2u){pk_bf16(v[0], v[1]), pk_bf16(v[2], v[3])};
      }
      asm volatile("" ::: "memory");
      char* ob = g_out + (size_t)mt * 16 * 2304 + 1152 + cg * 144;
#pragma unroll
      for (int j = 0; j < 3; ++j) {
        const unsigned c = j * 64 + lane, r = (c * 7282u) >> 16, k = c - r * 9u;
        if (c < 144) *(v4u*)(ob + r * 2304u + k * 16u) = lds16(sb + r * 176 + k * 16);
      }
    }
    if (blk == 0 && w == 4) {
#pragma unroll
      for (int j = 0; j < 4; ++j) poll_flags(g_ws + WS_F0 + j * 512, 64, tag, lane);
      if (lane == 0) flag_store(g_ws + WS_EPOCH, tag);
    }
  }
}

__global__ __launch_bounds__(64) void pad_tiny(char* ws) { const u64 t0 = __builtin_amdgcn_s_memrealtime(); while (__builtin_amdgcn_s_memrealtime() - t0 < 2000) __builtin_amdgcn_s_sleep(4); }
__global__ __launch_bounds__(512, 2) void pad_fat(char* ws) { const u64 t0 = __builtin_amdgcn_s_memrealtime(); asm volatile("v_mov_b32 v250, 0" ::: "v250");
  if (threadIdx.x >= 448 && blockIdx.x < 8) { while (__builtin_amdgcn_s_memrealtime() - t0 < 2000) __builtin_amdgcn_s_sleep(4); } }
extern "C" void kernel_launch(void* const* d_in, const int* in_sizes, int n_in, void* d_out, int out_size, void* d_ws, size_t ws_size,
                              hipStream_t stream) {
  (void)in_sizes; (void)n_in; (void)out_size; (void)ws_size;
  (void)hipFuncSetAttribute((const void*)fused_kernel, hipFuncAttributeMaxDynamicSharedMemorySize, LDS_BYTES);
  fused_kernel<<<256, 512, LDS_BYTES, stream>>>((const char*)d_in[0], (const char*)d_in[1], (const char*)d_in[2], (const char*)d_in[3],
                                                (const char*)d_in[4], (const char*)d_in[5], (const char*)d_in[6], (const char*)d_in[7],
                                                (const char*)d_in[8], (const char*)d_in[9], (const char*)d_in[10], (char*)d_out, (char*)d_ws);
  (void)hipFuncSetAttribute((const void*)pad_fat, hipFuncAttributeMaxDynamicSharedMemorySize, LDS_BYTES);
  pad_fat<<<256, 512, LDS_BYTES, stream>>>((char*)d_ws);
}
```

```cpp
#include <hip/hip_runtime.h>
#include <type_traits>

typedef unsigned short u16;
typedef unsigned long long u64;
typedef unsigned v4u __attribute__((ext_vector_type(4)));
typedef unsigned v3u __attribute__((ext_vector_type(3)));
typedef unsigned v2u __attribute__((ext_vector_type(2)));
typedef float f32x4 __attribute__((ext_vector_type(4)));
typedef float f32x2 __attribute__((ext_vector_type(2)));
typedef short s16x4 __attribute__((ext_vector_type(4)));
typedef __bf16 bf16x8 __attribute__((ext_vector_type(8)));
typedef __bf16 bf16x2 __attribute__((ext_vector_type(2)));
typedef _Float16 f16x8 __attribute__((ext_vector_type(8)));
typedef _Float16 f16x2 __attribute__((ext_vector_type(2)));
#define LAS __attribute__((address_space(3)))
#define GAS __attribute__((address_space(1)))
#define DI __device__ __forceinline__
typedef LAS char* lptr;

constexpr int WS_EPOCH = 0;
constexpr int WS_FC = 8192;
constexpr int WS_FT = 4096;
constexpr int WS_PART = 16384;
constexpr int PART_B = 5632;
constexpr int WS_OM = 1 << 20;
constexpr int WS_OS = 3 << 20;
constexpr int WS_UG = 4 << 20;
constexpr int WS_AG = 7 << 20;
constexpr int WS_AX = 9 << 20;
constexpr int WS_DBG = 10 << 20;
constexpr int WS_USED = 11 << 20;
constexpr int LDS_BYTES = 160 * 1024;
constexpr int CODE_BYTES = 56 * 1024;
constexpr unsigned EMPTY = 0xffffffffu;

constexpr int L_WO = 0, LB_IMG = 90112, LB_WPHI = 147456, LB_KVF = 155648;
constexpr int wslot(int t) { return (t & 1) ? 65536 : 90112; }
constexpr int xslot(int t) { return t % 3 == 2 ? 114688 : (t % 3) * 32768; }
constexpr int KV_EM = 4096, KV_NX = 5120, KV_EX = 5632, KV_END = 5760;
constexpr int L_RED = 110592, L_OST = L_RED + 20480;
constexpr int LA_KC = 4160, LA_UC = 4640, LA_U0 = 4 * LA_KC, LA_CH = 4 * LA_KC + 2 * LA_UC;
constexpr int LA_HS = 5 * LA_CH, LA_HSW = 8448;
constexpr int LA_OUT = LA_HS, LA_XP = LA_OUT + 4 * 4128, LA_FIMG = 90112;

#define STAMP_INS "s_memrealtime"
#define PROFW 0
#define STAMP(i) do {} while (0)

DI float bf2f(unsigned lo16) { return __uint_as_float(lo16 << 16); }
DI unsigned pk_bf16(float a, float b) { f32x2 v = {a, b}; return __builtin_bit_cast(unsigned, __builtin_convertvector(v, bf16x2)); }
DI unsigned pk_f16(float a, float b) { f32x2 v = {a, b}; return __builtin_bit_cast(unsigned, __builtin_convertvector(v, f16x2)); }
DI float rnd_bf16(float a) { return bf2f(pk_bf16(a, a) & 0xffffu); }
DI f32x4 mfma_bf(v4u a, v4u b, f32x4 c) { return __builtin_amdgcn_mfma_f32_16x16x32_bf16(__builtin_bit_cast(bf16x8, a), __builtin_bit_cast(bf16x8, b), c, 0, 0, 0); }
DI void mfma_acc(f32x4& c, v4u a, v4u b) { asm volatile("v_mfma_f32_16x16x32_bf16 %0, %1, %2, %0" : "+v"(c) : "v"(a), "v"(b)); }
#define MFMA_FENCE() do { __builtin_amdgcn_sched_barrier(0); asm volatile("s_nop 7" ::: "memory"); __builtin_amdgcn_sched_barrier(0); } while (0)
DI f32x4 mfma_h(v4u a, v4u b, f32x4 c) { return __builtin_amdgcn_mfma_f32_16x16x32_f16(__builtin_bit_cast(f16x8, a), __builtin_bit_cast(f16x8, b), c, 0, 0, 0); }
DI v2u trd(lptr p) { return __builtin_bit_cast(v2u, __builtin_amdgcn_ds_read_tr16_b64_v4i16((LAS s16x4*)p)); }
DI v4u trd2(lptr p0, lptr p1) { v2u a = trd(p0), b = trd(p1); return (v4u){a.x, a.y, b.x, b.y}; }
DI v4u lds16(lptr p) { return *(LAS v4u*)p; }
template <int IMM> DI void glds(const char* base, unsigned voff, unsigned lbase) {
  asm volatile("s_add_u32 m0, %2, %3\n\ts_nop 0\n\tglobal_load_lds_dwordx4 %0, %1" :: "v"(voff), "s"(base), "s"(lbase), "i"(IMM) : "memory", "scc");
}
#define IC(n) std::integral_constant<int, (n)>{}
template <int N, int I = 0, class F> DI void static_for(F&& f) { if constexpr (I < N) { f(std::integral_constant<int, I>{}); static_for<N, I + 1>(f); } }
DI void gld32(unsigned& d, const char* base, unsigned voff) { asm volatile("global_load_dword %0, %1, %2" : "=v"(d) : "v"(voff), "s"(base) : "memory"); }
DI void gld32p(unsigned& d, const char* p) { asm volatile("global_load_dword %0, %1, off" : "=v"(d) : "v"(p) : "memory"); }
DI void gld16(unsigned& d, const char* base, unsigned voff) { asm volatile("global_load_ushort %0, %1, %2" : "=v"(d) : "v"(voff), "s"(base) : "memory"); }
DI void gld96(v3u& d, const char* base, unsigned voff) { asm volatile("global_load_dwordx3 %0, %1, %2" : "=v"(d) : "v"(voff), "s"(base) : "memory"); }
#define WAITCNT(vm, lgkm) __builtin_amdgcn_s_waitcnt(((vm) & 15) | (((vm) >> 4) << 14) | 0x70 | ((lgkm) << 8))
#define BAR() __builtin_amdgcn_s_barrier()
#define SCHED() __builtin_amdgcn_sched_barrier(0)
#define DRAIN() asm volatile("s_waitcnt vmcnt(0)" ::: "memory")
DI void lds_barrier() { asm volatile("" ::: "memory"); WAITCNT(63, 0); BAR(); asm volatile("" ::: "memory"); }
DI f32x4 sel4(bool c, f32x4 a, f32x4 b) { return (f32x4){c ? a[0] : b[0], c ? a[1] : b[1], c ? a[2] : b[2], c ? a[3] : b[3]}; }
DI void ds_write2(unsigned addr, unsigned a, unsigned b) { asm volatile("ds_write2_b32 %0, %1, %2 offset1:1" :: "v"(addr), "v"(a), "v"(b) : "memory"); }

DI u64 mix64(u64 z) { z += 0x9E3779B97F4A7C15ull; z = (z ^ (z >> 30)) * 0xBF58476D1CE4E5B9ull; z = (z ^ (z >> 27)) * 0x94D049BB133111EBull; return z ^ (z >> 31); }
DI u64 read_tag(const char* ws) {
  u64 e; asm volatile("s_load_dwordx2 %0, %1, 0x0\n\ts_waitcnt lgkmcnt(0)" : "=s"(e) : "s"(ws) : "memory");
  return mix64(e);
}
DI void flag_store(char* p, u64 tag) { __hip_atomic_store((GAS u64*)p, tag, __ATOMIC_RELAXED, __HIP_MEMORY_SCOPE_AGENT); }
constexpr unsigned SPIN_CAP = 1u << 20;
DI void wait_tag(__amdgpu_buffer_rsrc_t rs, unsigned off, u64 tag) {
  for (unsigned spins = 0; spins < SPIN_CAP; ++spins) {
    const v2u v = __builtin_amdgcn_raw_buffer_load_b64(rs, off, 0, 16);
    if (__all(v.x == (unsigned)tag && v.y == (unsigned)(tag >> 32))) break;
    __builtin_amdgcn_s_sleep(1);
    asm volatile("" ::: "memory");
  }
  asm volatile("" ::: "memory");
}
DI void wait_word(__amdgpu_buffer_rsrc_t rs, unsigned off, unsigned& spins) {
  for (; spins < SPIN_CAP; ++spins) {
    if (__all(__builtin_amdgcn_raw_buffer_load_b32(rs, off, 0, 16) != EMPTY)) break;
    __builtin_amdgcn_s_sleep(1);
    asm volatile("" ::: "memory");
  }
  ++spins;
  asm volatile("" ::: "memory");
}
DI unsigned umax3(unsigned a, unsigned b, unsigned c) { return max(max(a, b), c); }
DI unsigned umax4(const v4u& v, unsigned m) { return umax3(umax3(v.x, v.y, v.z), v.w, m); }
DI unsigned nz(unsigned v) { return min(v, EMPTY - 1u); }
DI v4u nz4(v4u v) { return (v4u){nz(v.x), nz(v.y), nz(v.z), nz(v.w)}; }

DI void swap16(float& a, float& b) { auto r = __builtin_amdgcn_permlane16_swap(__float_as_uint(a), __float_as_uint(b), false, false); a = __uint_as_float(r[0]); b = __uint_as_float(r[1]); }
DI float mx3(float a, float b, float c) { return __builtin_elementwise_maximum(__builtin_elementwise_maximum(a, b), c); }
DI float mn3(float a, float b, float c) { return __builtin_elementwise_minimum(__builtin_elementwise_minimum(a, b), c); }
DI f32x4 mfma_bf16k(v2u a, v2u b, f32x4 c) { return mfma_bf((v4u){a.x, a.y, 0u, 0u}, (v4u){b.x, b.y, 0u, 0u}, c); }

DI void features(const f32x4& F0, const f32x4& F1, float F8, unsigned (&P)[9]) {
  const float L = 1.4426950408889634f;
  const float F[9] = {F0[0], F0[1], F0[2], F0[3], F1[0], F1[1], F1[2], F1[3], F8};
  float m = mx3(mx3(F[0], F[1], F[2]), mx3(F[3], F[4], F[5]), mx3(F[6], F[7], F[8]));
  float n = -mn3(mn3(F[0], F[1], F[2]), mn3(F[3], F[4], F[5]), mn3(F[6], F[7], F[8]));
  swap16(m, n);
  float t1 = __builtin_elementwise_maximum(m, n) * -L, t2 = t1;
  swap16(t1, t2);
  const f32x2 off = {t1, t2}, LL = {L, -L};
  f32x2 e[9];
#pragma unroll
  for (int j = 0; j < 9; ++j) {
    const f32x2 x = __builtin_elementwise_fma((f32x2){F[j], F[j]}, LL, off);
    e[j] = (f32x2){__builtin_amdgcn_exp2f(x[0]), __builtin_amdgcn_exp2f(x[1])};
  }
  const f32x2 sm = ((e[0] + e[1]) + (e[2] + e[3])) + ((e[4] + e[5]) + (e[6] + e[7])) + e[8];
  float s0 = sm[0], s1 = sm[1];
  swap16(s0, s1);
  float r1 = __builtin_amdgcn_rcpf(s0 + s1), r2 = r1;
  swap16(r1, r2);
  const f32x2 r = {r1, r2};
#pragma unroll
  for (int j = 0; j < 9; ++j) { const f32x2 v = e[j] * r; P[j] = pk_bf16(v[0], v[1]); }
}

struct cplx2 { f32x2 r, i; };
DI cplx2 cmul(const cplx2& a, const cplx2& b) { cplx2 c; c.r = a.r * b.r - a.i * b.i; c.i = a.r * b.i + a.i * b.r; return c; }
DI cplx2 cexp2(f32x2 al, f32x2 wr, float p) {
  cplx2 c;
#pragma unroll
  for (int k = 0; k < 2; ++k) {
    float m = __builtin_amdgcn_exp2f(al[k] * p), f = __builtin_amdgcn_fractf(wr[k] * p);
    c.r[k] = m * __builtin_amdgcn_cosf(f); c.i[k] = m * __builtin_amdgcn_sinf(f);
  }
  return c;
}
DI void put_row(lptr p, const cplx2& v) { *(LAS v2u*)p = (v2u){pk_f16(v.r[0], v.i[0]), pk_f16(v.r[1], v.i[1])}; }

template <int IMM> DI void wo_piece(const char* wob, int q, unsigned lbase, int lane) {
  int ln = lane; asm volatile("" : "+v"(ln));
  const unsigned c = q * 64 + ln, r = (c * 7282u) >> 16;
  glds<IMM>(wob, r * 1008u + c * 16u, lbase);
}
DI float exp_acc(float x) {
  const float e = __builtin_amdgcn_exp2f(x * 1.44268798828125f);
  return fmaf(e, x * 4.888496058e-06f, e);
}
template <int CTRL> DI unsigned dpp_umax(unsigned v) { return max(v, (unsigned)__builtin_amdgcn_update_dpp(0u, v, CTRL, 0xf, 0xf, true)); }

struct Modes { f32x2 al, wr; cplx2 C; float kscale; };
DI Modes prep_modes(const unsigned (&q)[4], unsigned ldt) {
  Modes M;
  const float dt = rnd_bf16(exp_acc(__uint_as_float(ldt)));
  const unsigned ar = pk_bf16(exp_acc(bf2f(q[0])), exp_acc(__uint_as_float(q[0] & 0xffff0000u)));
  M.al = (f32x2){bf2f(ar), __uint_as_float(ar & 0xffff0000u)} * dt * -1.4426950408889634f;
  M.wr = (f32x2){bf2f(q[1]), __uint_as_float(q[1] & 0xffff0000u)} * dt * 0.15915494309189535f;
  M.C.r = (f32x2){bf2f(q[2]), __uint_as_float(q[2] & 0xffff0000u)}; M.C.i = (f32x2){bf2f(q[3]), __uint_as_float(q[3] & 0xffff0000u)};
  unsigned mx = max(max((q[2] << 16) & 0x7fffffffu, q[2] & 0x7fffffffu), max((q[3] << 16) & 0x7fffffffu, q[3] & 0x7fffffffu));
  mx = dpp_umax<0x111>(mx); mx = dpp_umax<0x112>(mx); mx = dpp_umax<0x114>(mx); mx = dpp_umax<0x118>(mx); mx = dpp_umax<0x142>(mx); mx = dpp_umax<0x143>(mx);
  int ex = __builtin_amdgcn_readlane(mx, 63) >> 23; ex = ex < 30 ? 30 : (ex > 230 ? 230 : ex);
  const float sc = __uint_as_float((unsigned)(267 - ex) << 23);
  M.kscale = 2.f * dt * __uint_as_float((unsigned)(ex - 28) << 23);
  M.C.r *= sc; M.C.i *= sc;
  return M;
}
template <int N, int S> DI void gen_run(lptr p, cplx2 x, const cplx2& step) {
#pragma unroll
  for (int r = 0; r < N; ++r) { put_row(p + r * S, x); if (r + 1 < N) x = cmul(x, step); }
}
template <int S> DI void gen_y(lptr p, const Modes& M) {
  cplx2 e = cexp2(M.al, M.wr, 16.f); e.i = -e.i;
  cplx2 y; y.r = (f32x2){32768.f, 32768.f}; y.i = (f32x2){0.f, 0.f};
  put_row(p, y); y.r = e.r * 32768.f; y.i = e.i * 32768.f;
  gen_run<15, S>(p + S, y, e);
}
DI void read8(v4u (&f)[8], lptr p) {
#pragma unroll
  for (int ks = 0; ks < 8; ++ks) f[ks] = lds16(p + ks * 64);
}
DI f32x4 dot8(const v4u (&a)[8], const v4u (&y)[8], float sc) {
  f32x4 k = {0.f, 0.f, 0.f, 0.f};
#pragma unroll
  for (int ks = 0; ks < 8; ++ks) k = mfma_h(a[ks], y[ks], k);
  return k * sc;
}
DI int row_t1(int s16) { const int r = 15 - s16; return (r + ((r >> 1) & 1)) >> 1; }
DI int row_t23(int tq, int tp) { return 5 * (tp >> 1) + 4 - tq - (tp & 1); }
DI int row_t47(int tq, int tp) { const int rg = 3 - tq; return 3 * (tp >> 1) + ((tp & 1) ? (rg == 3 ? 2 : rg >> 1) : (rg + 1) >> 1); }
DI v2u pack_4(const f32x4& k) { return (v2u){pk_bf16(k[0], k[1]), pk_bf16(k[2], k[3])}; }
DI v2u pack_s4(float a, float b, bool odd) { return (v2u){pk_bf16(a, odd ? b : a), pk_bf16(b, b)}; }
DI v2u pack_s8(float a, float b) { return (v2u){pk_bf16(a, a), pk_bf16(a, b)}; }
template <int T> DI void put_k2(const unsigned (&ka)[8], const v2u& a, const v2u& b) {
#pragma unroll
  for (int k = 0; k < 4; ++k)
    asm volatile("ds_write2st64_b32 %0, %2, %4 offset0:%6 offset1:%7\n\tds_write2st64_b32 %1, %3, %5 offset0:%6 offset1:%7"
                 :: "v"(ka[k]), "v"(ka[4 + k]), "v"(a.x), "v"(a.y), "v"(b.x), "v"(b.y), "i"(2 * (7 - T)), "i"(2 * (6 - T)) : "memory");
}
template <int T> DI void put_k1(const unsigned (&ka)[8], const v2u& a) {
#pragma unroll
  for (int k = 0; k < 4; ++k) asm volatile("ds_write_b32 %0, %1 offset:%3\n\tds_write_b32 %0, %2 offset:%4" :: "v"(ka[k]), "v"(a.x), "v"(a.y), "i"(512 * (7 - T)), "i"(512 * (7 - T) + 4) : "memory");
}

__global__ __launch_bounds__(512, 2) void fused_kernel(
    const char* __restrict__ g_u, const char* __restrict__ g_lar, const char* __restrict__ g_aim, const char* __restrict__ g_cr,
    const char* __restrict__ g_ci, const char* __restrict__ g_ldt, const char* __restrict__ g_dsk, const char* __restrict__ g_x,
    const char* __restrict__ g_w, const char* __restrict__ g_wphi, const char* __restrict__ g_wo, char* __restrict__ g_out, char* g_ws) {
  extern __shared__ __attribute__((aligned(16))) char smem[];
  const lptr lds = (lptr)smem;
  const int tid = threadIdx.x, lane = tid & 63, w = __builtin_amdgcn_readfirstlane(tid >> 6) & 7;
  const int blk = blockIdx.x, xcd = blk & 7, bi = blk >> 3;
  const int g = lane >> 4, s16 = lane & 15, tq = (lane >> 2) & 3, tp = lane & 3;
  const int cg = bi & 7;
  const unsigned lw1k = (unsigned)(size_t)lds + w * 1024;
  STAMP(0);
  const char* code0; asm volatile("s_getpc_b64 %0" : "=s"(code0));
  asm volatile("" :: "s"(g_u), "s"(g_lar), "s"(g_aim), "s"(g_cr), "s"(g_ci), "s"(g_ldt), "s"(g_dsk), "s"(g_x), "s"(g_w), "s"(g_wphi), "s"(g_wo), "s"(g_out), "s"(g_ws));
  const __amdgpu_buffer_rsrc_t ws_rs = __builtin_amdgcn_make_buffer_rsrc(g_ws, 0, WS_USED, 0x00020000);
  const char* wob = g_wo + cg * 144;
  u64 tag = 0;

  if (bi < 16) {
    const int rb = (xcd >> 1) * 2 + (bi >> 3), hp = (xcd & 1) * 8 + (bi & 7);
    if (w >= 4) {
      const int lw = w - 4, lr = lane >> 3, lc = lane & 7;
      unsigned w_off0 = (16 * lw + lr) * 3456u + (((72 * hp) & ~63) + ((lc ^ (lr & 2)) << 4));
      unsigned w_off1 = (16 * lw + 8 + lr) * 3456u + (((72 * hp) & ~63) + ((lc ^ ((lr & 2) | 4)) << 4));
      const char* xb0 = g_x + (size_t)(rb * 256 + lw * 64) * 1152;
      const char *xb1 = xb0 + 16 * 1152, *xb2 = xb0 + 32 * 1152, *xb3 = xb0 + 48 * 1152, *wb1 = g_w + 1152, *wb2 = g_w + 2304;
      unsigned x_off0 = lr * 1152u + ((lc ^ g) << 4), x_off1 = (8 + lr) * 1152u + ((lc ^ (4 + g)) << 4);
      const unsigned l2k = (unsigned)(size_t)lds + lw * 2048, l8k = (unsigned)(size_t)lds + lw * 8192;
      auto issue_w = [&](auto T) {
        constexpr int o = wslot(T);
        glds<o>(g_w, w_off0, l2k); glds<o + 1024>(g_w, w_off1, l2k); glds<o + 8192>(wb1, w_off0, l2k); glds<o + 9216>(wb1, w_off1, l2k);
        glds<o + 16384>(wb2, w_off0, l2k); glds<o + 17408>(wb2, w_off1, l2k);
        w_off0 += 64 * 3456; w_off1 += 64 * 3456; asm volatile("" : "+v"(w_off0), "+v"(w_off1));
      };
      auto issue_xa = [&](auto T) {
        constexpr int o = xslot(T);
        glds<o>(xb0, x_off0, l8k); glds<o + 1024>(xb0, x_off1, l8k); glds<o + 2048>(xb1, x_off0, l8k); glds<o + 3072>(xb1, x_off1, l8k);
      };
      auto issue_xb = [&](auto T) {
        constexpr int o = xslot(T);
        glds<o + 4096>(xb2, x_off0, l8k); glds<o + 5120>(xb2, x_off1, l8k); glds<o + 6144>(xb3, x_off0, l8k); glds<o + 7168>(xb3, x_off1, l8k);
        x_off0 += 128; x_off1 += 128; asm volatile("" : "+v"(x_off0), "+v"(x_off1));
      };
      issue_xa(IC(0)); issue_xb(IC(0)); issue_w(IC(0));
      BAR();
      issue_xa(IC(1));
      asm volatile("s_waitcnt vmcnt(4)" ::: "memory"); BAR();
      issue_xb(IC(1));
      static_for<9>([&](auto T) {
        constexpr int t = T;
        if constexpr (t < 8) issue_w(IC(t + 1));
        if constexpr (t < 7) { issue_xa(IC(t + 2)); issue_xb(IC(t + 2)); }
        if constexpr (t == 7) static_for<8>([&](auto I) { wo_piece<L_WO + I * 4096 - 4096>(wob, I * 4 + lw, lw1k, lane); });
        if constexpr (t == 8) {
          static_for<6>([&](auto I) { wo_piece<L_WO + (8 + I) * 4096 - 4096>(wob, (8 + I) * 4 + lw, lw1k, lane); });
          const lptr z = lds + LB_KVF + (tid - 256) * 16;
          *(LAS v4u*)z = (v4u){0u, 0u, 0u, 0u}; if (tid - 256 < (KV_END - 4096) / 16) *(LAS v4u*)(z + 4096) = (v4u){0u, 0u, 0u, 0u};
        }
        if constexpr (t < 8) asm volatile("s_waitcnt vmcnt(8)" ::: "memory");
        BAR();
      });
      static_for<7>([&](auto I) { if (I < 6 || lw == 0) wo_piece<L_WO + (14 + I) * 4096 - 4096>(wob, (14 + I) * 4 + lw, lw1k, lane); });
      tag = read_tag(g_ws);
      {
        const int h = lw >> 1;
        const lptr ib = lds + LB_IMG + (4 * g + tq) * 224;
        const lptr pa = ib + lw * 32 + ((tp ^ ((g & 1) << 1)) << 3), pe = ib + 128 + ((tp ^ g) << 3), pb = pe + 32 + 32 * h;
        f32x4 c0 = {0.f, 0.f, 0.f, 0.f}, c1 = c0, c2 = c0;
#pragma unroll
        for (int hf = 0; hf < 2; ++hf) {
          if (hf == 1 && w >= 6) wait_tag(ws_rs, WS_FC + (w == 6 ? ((lane >> 2 & 7) * 16 + hp) * 32 + tp * 8 : xcd * 512 + lane * 8), tag);
          lds_barrier();
          v4u A[4], B[4], E[4];
#pragma unroll
          for (int ks = 0; ks < 4; ++ks) {
            const int o = (ks * 64 + hf * 32) * 224;
            A[ks] = trd2(pa + o, pa + o + 16 * 224); B[ks] = trd2(pb + o, pb + o + 16 * 224); E[ks] = trd2(pe + o, pe + o + 16 * 224);
          }
          SCHED();
#pragma unroll
          for (int ks = 0; ks < 4; ++ks) { c0 = mfma_bf(A[ks], B[ks], c0); c1 = mfma_bf(A[ks], E[ks], c1); }
          if (lw & 1) {
#pragma unroll
            for (int ks = 0; ks < 4; ++ks) c2 = mfma_bf(E[ks], E[ks], c2);
          } else {
#pragma unroll
            for (int ks = 0; ks < 4; ++ks) c2 = mfma_bf(E[ks], B[ks], c2);
          }
        }
        const unsigned pbase = WS_PART + (hp * 8 + rb) * PART_B;
        const int rec = s16 == 12 ? 0 : s16 - 7 - 2 * h, rx = s16 == 12 ? 0 : s16 - 7 - 2 * g;
        const unsigned o1 = (s16 == 12 || rec == 1 || rec == 2) ? pbase + (256 + (lw * 3 + rec) * 4 + g) * 16 : WS_USED;
        const unsigned o2 = (lw & 1) ? ((lw == 1 && g < 2 && (s16 == 12 || rx == 1 || rx == 2)) ? pbase + (336 + g * 3 + rx) * 16 : WS_USED)
                                     : (g == h ? pbase + (304 + h * 16 + s16) * 16 : WS_USED);
        __builtin_amdgcn_raw_buffer_store_b128(nz4(__builtin_bit_cast(v4u, c1)), ws_rs, o1, 0, 16);
        __builtin_amdgcn_raw_buffer_store_b128(nz4(__builtin_bit_cast(v4u, c2)), ws_rs, o2, 0, 16);
        __builtin_amdgcn_raw_buffer_store_b128(nz4(__builtin_bit_cast(v4u, c0)), ws_rs, pbase + (lw * 64 + lane) * 16, 0, 16);
      }
      STAMP(4);
      const int t2 = tid - 256;
      const unsigned po = WS_PART + hp * 8 * PART_B + t2 * 16, po2 = t2 < 86 ? po + 4096 : WS_USED;
      int dst2;
      if (t2 < 48) { const int a = t2 / 12, r3 = (t2 >> 2) - a * 3, h = a >> 1, gq = t2 & 3; dst2 = KV_EM + (gq >> 1) * 512 + (r3 ? 1 + 2 * h + r3 : h) * 64 + a * 16 + (gq & 1) * 8; }
      else if (t2 < 80) { const int i = t2 - 48; dst2 = KV_NX + i * 16 + (i >> 4) * 8; }
      else { const int i = t2 - 80, h = i >= 3, r3 = i - 3 * h; dst2 = t2 < 86 ? KV_EX + (r3 ? 1 + 2 * h + r3 : h) * 16 + h * 8 : KV_END; }
      f32x4 sa, sb = {0.f, 0.f, 0.f, 0.f};
      for (unsigned spins = 0;;) {
        wait_word(ws_rs, WS_PART + (hp * 8 + (lane & 7)) * PART_B + lw * 1024 + 1020, spins);
        v4u pv[8];
        unsigned mx = 0;
#pragma unroll
        for (int r = 0; r < 8; ++r) pv[r] = __builtin_amdgcn_raw_buffer_load_b128(ws_rs, po, r * PART_B, 16);
        if (lw < 2) {
          v4u pw[8];
#pragma unroll
          for (int r = 0; r < 8; ++r) pw[r] = __builtin_amdgcn_raw_buffer_load_b128(ws_rs, po2, r * PART_B, 16);
          SCHED();
          sb = __builtin_bit_cast(f32x4, pw[0]);
#pragma unroll
          for (int r = 0; r < 8; ++r) { mx = umax4(pw[r], mx); if (r) sb += __builtin_bit_cast(f32x4, pw[r]); }
        }
        SCHED();
        sa = __builtin_bit_cast(f32x4, pv[0]);
#pragma unroll
        for (int r = 0; r < 8; ++r) { mx = umax4(pv[r], mx); if (r) sa += __builtin_bit_cast(f32x4, pv[r]); }
        if (__all(mx != EMPTY) || spins >= SPIN_CAP) break;
      }
      *(LAS v2u*)(lds + LB_KVF + (lw >> 1) * 2048 + (g >> 1) * 1024 + (lw * 16 + s16) * 16 + (g & 1) * 8) = pack_4(sa);
      if (lw < 2) *(LAS v2u*)(lds + LB_KVF + dst2) = pack_4(sb);
      DRAIN();
      STAMP(5);
      lds_barrier();
    } else {
      const unsigned l2k = (unsigned)(size_t)lds + w * 2048;
      unsigned cpf;
      { const unsigned i = (bi * 4 + w) * 9 + (lane < 9 ? lane : 8); gld32(cpf, code0, min(i * 128u, (unsigned)CODE_BYTES - 128u)); }
      glds<LB_WPHI>(g_wphi + hp * 1296, lane * 16, l2k); glds<LB_WPHI + 1024>(g_wphi + hp * 1296, (lane < 17 ? 64 + lane : 80) * 16, l2k);
      BAR();
      const int ub = ((72 * hp) & 63) >> 3, fsw = (tq & 2) | ((g & 1) << 2), trow = wslot(1) + (8 * g + tq) * 128;
      lptr wa[3];
      wa[0] = lds + trow + ((((ub + tp) >> 1) ^ fsw) << 4) + ((ub + tp) & 1) * 8;
      wa[1] = lds + trow + ((((ub + 5 + tp) >> 1) ^ fsw) << 4) + ((ub + 5 + tp) & 1) * 8;
      wa[2] = lds + trow + ((((ub + 4) >> 1) ^ fsw) << 4) + ((ub + 4) & 1) * 8 + (tp < 2 ? tp : 2) * 8192;
      lptr xa[2][2];
      xa[0][0] = lds + w * 8192 + s16 * 128 + ((g ^ (s16 >> 1)) << 4); xa[0][1] = lds + w * 8192 + s16 * 128 + (((4 + g) ^ (s16 >> 1)) << 4);
      xa[1][0] = xa[0][0] + xslot(2); xa[1][1] = xa[0][1] + xslot(2);
      asm volatile("" : "+v"(wa[0]), "+v"(wa[1]), "+v"(wa[2]), "+v"(xa[0][0]), "+v"(xa[0][1]), "+v"(xa[1][0]), "+v"(xa[1][1]));
      unsigned wm[3][2], wsp[3];
      {
        asm volatile("s_waitcnt vmcnt(0)" : "+v"(cpf) :: "memory");
        const int hh = s16 >> 3;
        const lptr wp = lds + LB_WPHI + w * 2048 + hh * 648, wd = wp + (4 * g + 2 * hh) * 36, wsd = wp + (hh ? 0 : 16 * 36);
#pragma unroll
        for (int i = 0; i < 3; ++i) {
          const int f0 = 9 * (tq & 1) + 4 * i + tp, f = (f0 < 17 ? f0 : 17) * 2;
          wm[i][0] = *(LAS u16*)(wd + f) | ((unsigned)*(LAS u16*)(wd + f + 36) << 16);
          wm[i][1] = *(LAS u16*)(wd + f + 72) | ((unsigned)*(LAS u16*)(wd + f + 108) << 16);
          wsp[i] = *(LAS u16*)(wsd + f) | ((unsigned)*(LAS u16*)(wsd + f + 36) << 16);
        }
        asm volatile("" : "+v"(wm[0][0]), "+v"(wm[0][1]), "+v"(wsp[0]), "+v"(wm[1][0]), "+v"(wm[1][1]), "+v"(wsp[1]), "+v"(wm[2][0]), "+v"(wm[2][1]), "+v"(wsp[2]));
      }
      auto read_f = [&](v2u (&wf)[14], v4u (&xf)[4], int t, int ks, int r) {
        if (r < 4) xf[r] = lds16(xa[t % 3 == 2][ks] + (t % 3 == 2 ? 0 : xslot(t)) + r * 2048);
        else if (r < 18) { const int n = (r - 4) >> 1; wf[r - 4] = trd(wa[n < 6 ? (n & 1) : 2] + (wslot(t) - wslot(1)) + ks * 4096 + (n < 6 ? (n >> 1) * 8192 : 0) + (r & 1) * 512); }
      };
      f32x4 acc[7][4];
#pragma unroll
      for (int n = 0; n < 7; ++n)
#pragma unroll
        for (int m = 0; m < 4; ++m) { acc[n][m] = (f32x4){0.f, 0.f, 0.f, 0.f}; asm volatile("" : "+v"(acc[n][m])); }
      v2u wf0[14], wf1[14]; v4u xf0[4], xf1[4];
      WAITCNT(63, 0); BAR(); asm volatile("" ::: "memory");
      STAMP(1);
#pragma unroll
      for (int r = 0; r < 18; ++r) read_f(wf0, xf0, 0, 0, r);
      static_for<9>([&](auto T) {
        constexpr int t = T;
        static_for<28>([&](auto I) {
          constexpr int n = I >> 2, m = I & 3;
          SCHED();
          mfma_acc(acc[n][m], (v4u){wf0[2 * n].x, wf0[2 * n].y, wf0[2 * n + 1].x, wf0[2 * n + 1].y}, xf0[m]);
          SCHED();
          if constexpr (I >= 4) read_f(wf1, xf1, t, 1, I - 4);
        });
        static_for<28>([&](auto I) {
          constexpr int n = I >> 2, m = I & 3;
          SCHED();
          if constexpr (I == 10) { asm volatile("" ::: "memory"); WAITCNT(63, 0); BAR(); asm volatile("" ::: "memory"); SCHED(); }
          mfma_acc(acc[n][m], (v4u){wf1[2 * n].x, wf1[2 * n].y, wf1[2 * n + 1].x, wf1[2 * n + 1].y}, xf1[m]);
          SCHED();
          if constexpr (t < 8 && I >= 10) read_f(wf0, xf0, t + 1, 0, I - 10);
        });
      });
      MFMA_FENCE();
      STAMP(2);
      const bool h1 = s16 >= 8;
      v4u Am[3], As[3];
#pragma unroll
      for (int i = 0; i < 3; ++i) Am[i] = (v4u){h1 ? 0u : wm[i][0], h1 ? 0u : wm[i][1], h1 ? wm[i][0] : 0u, h1 ? wm[i][1] : 0u};
      auto set_part = [&](int part) {
#pragma unroll
        for (int i = 0; i < 3; ++i) As[i] = (v4u){(g == part && !h1) ? wsp[i] : 0u, (g == part && h1) ? wsp[i] : 0u, 0u, 0u};
      };
      v2u Sp[4];
#pragma unroll
      for (int m = 0; m < 4; ++m) Sp[m] = pack_4(acc[6][m]);
      auto phi = [&](int part, int m, unsigned (&P)[9]) {
        const v2u b0 = pack_4(acc[2 * part][m]), b1 = pack_4(acc[2 * part + 1][m]);
        const v4u Bm = {b0.x, b0.y, b1.x, b1.y}, Bs = {Sp[m].x, Sp[m].y, Sp[m].x, Sp[m].y};
        f32x4 F[3];
#pragma unroll
        for (int i = 0; i < 3; ++i) F[i] = mfma_bf(As[i], Bs, mfma_bf(Am[i], Bm, (f32x4){0.f, 0.f, 0.f, 0.f}));
        features(F[0], F[1], F[2][0], P);
      };
      {
        const lptr row0 = lds + LB_IMG + (64 * w + s16) * 224;
        const lptr rt = row0 + g * 32 + ((tq & 1) << 4), rt2 = row0 + g * 32 + (((tq & 1) ^ 1) << 4), rp = row0 + ((g ^ tq) << 3);
        const lptr rx = row0 + 128 + (((g >> 1) ^ tq) << 3) + (g & 1) * 4;
        set_part(1);
        if (g >= 2) {
#pragma unroll
          for (int m = 0; m < 4; ++m) *(LAS v2u*)(rp + m * 16 * 224 + 128) = (v2u){g == 2 ? Sp[m].x : 0x3f803f80u, g == 2 ? Sp[m].y : 0x3f803f80u};
        }
#pragma unroll
        for (int hf = 0; hf < 2; ++hf) {
#pragma unroll
          for (int m = 2 * hf; m < 2 * hf + 2; ++m) {
            const int o = m * 16 * 224;
            unsigned P[9];
            phi(1, m, P);
            *(LAS v2u*)(rp + o + 160) = pack_4(acc[4][m]); *(LAS v2u*)(rp + o + 192) = pack_4(acc[5][m]);
            *(LAS v4u*)(rt + o) = (v4u){P[0], P[1], P[2], P[3]}; *(LAS v4u*)(rt2 + o) = (v4u){P[4], P[5], P[6], P[7]};
            *(LAS unsigned*)(rx + o) = P[8];
          }
          lds_barrier();
        }
      }
      STAMP(3);
      unsigned Q[4][9];
      set_part(0);
#pragma unroll
      for (int m = 0; m < 4; ++m) phi(0, m, Q[m]);
#pragma unroll
      for (int m = 0; m < 4; ++m) asm volatile("" : "+v"(Q[m][0]), "+v"(Q[m][1]), "+v"(Q[m][2]), "+v"(Q[m][3]), "+v"(Q[m][4]), "+v"(Q[m][5]), "+v"(Q[m][6]), "+v"(Q[m][7]), "+v"(Q[m][8]));
      STAMP(5);
      lds_barrier();
      STAMP(7);
      {
        const lptr kb = lds + LB_KVF;
        const int rec = tp < 2 ? tp : 2 * (tq & 1) + tp;
        v4u Nm[2][2], Em[2]; unsigned Nx[2], Ex;
#pragma unroll
        for (int h = 0; h < 2; ++h) Em[h] = lds16(kb + KV_EM + h * 512 + rec * 64 + g * 16);
        Ex = *(LAS unsigned*)(kb + KV_EX + rec * 16 + g * 4);
#pragma unroll
        for (int h = 0; h < 2; ++h) {
          Nm[h][0] = lds16(kb + h * 2048 + lane * 16); Nm[h][1] = lds16(kb + h * 2048 + 1024 + lane * 16);
          Nx[h] = *(LAS unsigned*)(kb + KV_NX + h * 256 + s16 * 16 + g * 4);
        }
        const f32x4 z = {0.f, 0.f, 0.f, 0.f};
        f32x4 E[4];
#pragma unroll
        for (int m = 0; m < 4; ++m)
          E[m] = mfma_bf((v4u){Ex, 0u, 0u, 0u}, (v4u){Q[m][8], Q[m][0], Q[m][1], Q[m][2]},
                 mfma_bf(Em[1], (v4u){Q[m][4], Q[m][5], Q[m][6], Q[m][7]}, mfma_bf(Em[0], (v4u){Q[m][0], Q[m][1], Q[m][2], Q[m][3]}, z)));
        unsigned sp[4];
#pragma unroll
        for (int m = 0; m < 4; ++m) {
          const v4u B0 = {Q[m][0], Q[m][1], Q[m][2], Q[m][3]}, B1 = {Q[m][4], Q[m][5], Q[m][6], Q[m][7]}, Bx = {Q[m][8], Q[m][0], Q[m][1], Q[m][2]};
          const f32x4 O0 = mfma_bf((v4u){Nx[0], 0u, 0u, 0u}, Bx, mfma_bf(Nm[0][1], B1, mfma_bf(Nm[0][0], B0, z)));
          const f32x4 O1 = mfma_bf((v4u){Nx[1], 0u, 0u, 0u}, Bx, mfma_bf(Nm[1][1], B1, mfma_bf(Nm[1][0], B0, z)));
          const float r0 = __builtin_amdgcn_rcpf(E[m][0] + 1e-6f), r1 = __builtin_amdgcn_rcpf(E[m][1] + 1e-6f), rs = (g & 1) ? r1 : r0;
          const v2u o0 = pack_4(O0 * r0), o1 = pack_4(O1 * r1);
          __builtin_amdgcn_raw_buffer_store_b128(nz4((v4u){o0.x, o0.y, o1.x, o1.y}), ws_rs, WS_OM + ((rb * 16 + 4 * w) * 16 + hp) * 1024 + lane * 16, m * 16384, 16);
          sp[m] = pk_bf16(E[m][2] * rs, E[m][3] * rs);
        }
        const unsigned so = WS_OS + ((rb * 16 + hp) * 256 + 64 * w + (g >> 1) * 16 + s16) * 8 + (g & 1) * 4;
        __builtin_amdgcn_raw_buffer_store_b32(nz(g < 2 ? sp[0] : sp[1]), ws_rs, so, 0, 16);
        __builtin_amdgcn_raw_buffer_store_b32(nz(g < 2 ? sp[2] : sp[3]), ws_rs, so, 256, 16);
      }
    }
    STAMP(8);
  } else {
    const int aj = bi - 16, ab = xcd * 16 + aj;
    const int ch = 72 * xcd + (w < 4 ? 4 * aj + w : 64 + (aj >> 1));
    const lptr img = lds + (w < 4 ? w : 4) * LA_CH;
    unsigned ka[8];
    auto set_ka = [&]() {
      const unsigned kb = (unsigned)(size_t)img + 16 + 2 * (2032 - 6 - 1792 - 16 * s16 + 4 * g);
#pragma unroll
      for (int k = 0; k < 4; ++k) { ka[k] = kb + ((k + tq) & 3) * (LA_KC + 4); ka[4 + k] = ka[k] + 4; }
    };
    unsigned prm[4], pdt, pds;
    f32x4 y[8];
    auto load_prm = [&]() {
      const unsigned o = ch * 256 + lane * 4;
      gld32(prm[0], g_lar, o); gld32(prm[1], g_aim, o); gld32(prm[2], g_cr, o); gld32(prm[3], g_ci, o);
    };
    lptr kp = img + tp * LA_KC + 16 + 2 * (2040 - 1792 - 224 + 8 * (g - tq));
    lptr up = img + LA_U0 + (s16 & 1) * LA_UC + 2 * (32 * (s16 >> 1) + 8 * g);
    if (w < 4) {
      unsigned pf[3];
      const unsigned pi = aj * 256 + tid;
      auto prefetch = [&](auto J) {
        if constexpr (J == 1) gld32(pf[1], g_x + (xcd >> 1) * 512 * 1152 + 128, (pi & 511u) * 1152u + (pi >> 9) * 128u);
        else { const unsigned q = min(pi & 15u, 14u); gld32(pf[J], g_w + (64 + J * 128) * 3456 + (xcd & 1) * 512, (pi >> 4) * 3456u + (q * 13u >> 6) * 512u + q * 128u); }
      };
      load_prm();
      BAR();
      prefetch(IC(0)); prefetch(IC(1));
      asm volatile("s_load_dword %0, %2, %4\n\ts_load_dword %1, %3, %4\n\ts_waitcnt lgkmcnt(0)" : "=&s"(pdt), "=&s"(pds) : "s"(g_ldt), "s"(g_dsk), "s"((ch >> 1) * 4) : "memory");
      pdt = (ch & 1) ? pdt & 0xffff0000u : pdt << 16; pds = (ch & 1) ? pds & 0xffff0000u : pds << 16;
      asm volatile("s_waitcnt vmcnt(2)" : "+v"(prm[0]), "+v"(prm[1]), "+v"(prm[2]), "+v"(prm[3]) :: "memory");
      STAMP(6);
      const Modes M = prep_modes(prm, pdt);
      const lptr st = img + lane * 8, rd = img + g * 16;
      const cplx2 e1 = cexp2(M.al, M.wr, 1.f), e256 = cexp2(M.al, M.wr, 256.f), e2 = cmul(e1, e1), e4 = cmul(e2, e2), e8 = cmul(e4, e4);
      const cplx2 b1 = cmul(M.C, e256), b2 = cmul(b1, e256), b3 = cmul(b2, e256);
      v4u Y[8], A[8], B[8];
      v2u kd[8];
      gen_y<544>(st, M);
      { cplx2 b = b3;
#pragma unroll
        for (int t = 0; t < 4; ++t) { b = cmul(b, e256); gen_run<3, 544>(st + (16 + 3 * t) * 544, b, e8); } }
      asm volatile("" ::: "memory");
      read8(Y, rd + s16 * 544); read8(A, rd + (16 + row_t47(tq, tp)) * 544); read8(B, rd + (22 + row_t47(tq, tp)) * 544);
      asm volatile("" ::: "memory");
      prefetch(IC(2));
      gen_run<16, 544>(st, M.C, e1); gen_run<5, 544>(st + 16 * 544, b2, e4); gen_run<5, 544>(st + 21 * 544, b3, e4);
      { const f32x4 k0 = dot8(A, Y, M.kscale), k1 = dot8(B, Y, M.kscale);
        kd[4] = pack_s8(k0[0], k0[1]); kd[5] = pack_s8(k0[2], k0[3]); kd[6] = pack_s8(k1[0], k1[1]); kd[7] = pack_s8(k1[2], k1[3]); }
      asm volatile("" ::: "memory");
      read8(A, rd + (15 - s16) * 544); read8(B, rd + (16 + row_t23(tq, tp)) * 544);
      asm volatile("" ::: "memory");
      gen_run<9, 544>(st, b1, e2);
      { f32x4 k0 = dot8(A, Y, M.kscale); const f32x4 k1 = dot8(B, Y, M.kscale);
        if (lane == 48) k0[3] += __uint_as_float(pds);
        kd[0] = pack_4(k0); kd[2] = pack_s4(k1[0], k1[1], g & 1); kd[3] = pack_s4(k1[2], k1[3], g & 1); }
      asm volatile("" ::: "memory");
      read8(A, rd + row_t1(s16) * 544);
      asm volatile("" ::: "memory");
      STAMP(1);
      set_ka();
      put_k2<6>(ka, kd[6], kd[7]); put_k2<4>(ka, kd[4], kd[5]); put_k2<2>(ka, kd[2], kd[3]);
      kd[1] = pack_4(dot8(A, Y, M.kscale));
      put_k2<0>(ka, kd[0], kd[1]);
      if (s16 < 15 - g) *(LAS unsigned*)(img + g * LA_KC + 16 + 2 * (2042 + 2 * g) + s16 * 4) = 0u;
      asm volatile("s_waitcnt vmcnt(0)" : "+v"(pf[0]), "+v"(pf[1]), "+v"(pf[2]) :: "memory");
      STAMP(7);
      lds_barrier();
      STAMP(2);
#pragma unroll
      for (int t = 0; t < 8; ++t) { y[t] = (f32x4){0.f, 0.f, 0.f, 0.f}; asm volatile("" : "+v"(y[t])); }
      asm volatile("" : "+v"(kp), "+v"(up));
      v4u Kf[2][8], Uf[2][8];
#pragma unroll
      for (int d = 0; d < 8; ++d) { Uf[0][d] = lds16(up + 512 * d); Kf[0][d] = lds16(kp + 512 * (7 - d)); }
#pragma unroll 1
      for (int sp = 0; sp < 4; ++sp) {
        static_for<2>([&](auto CU) {
          constexpr int cu = CU, nx = cu ^ 1;
          static_for<8>([&](auto TT) {
            constexpr int t = TT;
            static_for<t + 1>([&](auto DD) {
              constexpr int d = DD, i = t * (t + 1) / 2 + d;
              SCHED();
              mfma_acc(y[t], Uf[cu][t - d], Kf[cu][d]);
              SCHED();
              if constexpr ((i & 1) == 1 && i < 32) {
                constexpr int f = i >> 2;
                if constexpr ((i & 2) == 0) Uf[nx][f] = lds16(up + 64 * (cu + 1) + 512 * f); else Kf[nx][f] = lds16(kp + 64 * (cu + 1) + 512 * (7 - f));
              }
            });
          });
        });
        kp += 128; up += 128;
      }
      MFMA_FENCE();
      const unsigned ob = (unsigned)(size_t)(lds + LA_OUT + w * 4128) + 4 * (32 * g + s16);
#pragma unroll
      for (int t = 0; t < 8; t += 2) {
        const v2u a = pack_4(y[t]), b = pack_4(y[t + 1]);
        asm volatile("ds_write2_b32 %0, %1, %2 offset0:0 offset1:16\n\tds_write2_b32 %0, %3, %4 offset0:128 offset1:144" :: "v"(ob + 512 * t), "v"(a.x), "v"(a.y), "v"(b.x), "v"(b.y) : "memory");
      }
    } else {
      const int h = w - 4, t2 = tid - 256;
      const lptr hs = lds + LA_HS + h * LA_HSW;
      __builtin_amdgcn_s_setprio(2);
      {
        const unsigned lh = (unsigned)(size_t)lds + h * LA_HSW;
        const char* ubase = g_u + (size_t)(128 * aj + 32 * h) * 1152 + 144 * xcd;
        static_for<5>([&](auto I) { const unsigned c0 = I * 64 + lane, c = c0 < 288u ? c0 : 287u, r = (c * 7282u) >> 16; glds<LA_HS + I * 1024>(ubase, r * 1008u + c * 16u, lh); });
      }
      load_prm();
      BAR();
      asm volatile("s_load_dword %0, %2, %4\n\ts_load_dword %1, %3, %4\n\ts_waitcnt lgkmcnt(0)" : "=&s"(pdt), "=&s"(pds) : "s"(g_ldt), "s"(g_dsk), "s"((ch >> 1) * 4) : "memory");
      pdt = (ch & 1) ? pdt & 0xffff0000u : pdt << 16; pds = (ch & 1) ? pds & 0xffff0000u : pds << 16;
      tag = read_tag(g_ws);
      asm volatile("s_waitcnt vmcnt(0)" : "+v"(prm[0]), "+v"(prm[1]), "+v"(prm[2]), "+v"(prm[3]) :: "memory");
      STAMP(6);
      {
        const lptr tb = hs + (8 * tp + tq) * 144 + g * 8;
        const unsigned uo = WS_UG + (18 * xcd + g) * 16384 + (128 * aj + 32 * h) * 8 + s16 * 16;
        v4u tv[5];
#pragma unroll
        for (int i = 0; i < 5; ++i) tv[i] = trd2(tb + i * 32, tb + i * 32 + 4 * 144);
#pragma unroll
        for (int i = 0; i < 4; ++i) __builtin_amdgcn_raw_buffer_store_b128(tv[i], ws_rs, uo, i * 65536, 16);
        __builtin_amdgcn_raw_buffer_store_b128(tv[4], ws_rs, g < 2 ? uo + 4 * 65536 : WS_USED, 0, 16);
        DRAIN();
        if (lane == 0) flag_store(g_ws + WS_FT + (ab * 4 + h) * 8, tag);
      }
      STAMP(1);
      {
        const v4u em = {EMPTY, EMPTY, EMPTY, EMPTY};
        const unsigned o1 = WS_OM + ((xcd * 16 + 4 * h) * 16 + aj) * 1024 + lane * 16, o2 = WS_AG + ab * 16384 + t2 * 16, o3 = WS_PART + (aj * 8 + xcd) * PART_B + t2 * 16;
        const int xt = (aj & 1) ? (h == 0 ? 6 : h == 1 ? 5 : h == 2 ? 2 : 1) : (h == 0 ? 7 : h == 1 ? 4 : h == 2 ? 3 : 0);
        const unsigned o4 = lane < 32 ? WS_OS + ab * 2048 + h * 512 + lane * 16 : WS_AX + (xcd * 8 + (aj >> 1)) * 4096 + xt * 512 + (lane - 32) * 16;
#pragma unroll
        for (int i = 0; i < 4; ++i) { __builtin_amdgcn_raw_buffer_store_b128(em, ws_rs, o1, i * 16384, 16); __builtin_amdgcn_raw_buffer_store_b128(em, ws_rs, o2, i * 4096, 16); }
        __builtin_amdgcn_raw_buffer_store_b128(em, ws_rs, o3, 0, 16);
        __builtin_amdgcn_raw_buffer_store_b128(em, ws_rs, t2 < 96 ? o3 + 4096 : WS_USED, 0, 16);
        __builtin_amdgcn_raw_buffer_store_b128(em, ws_rs, o4, 0, 16);
      }
      __builtin_amdgcn_s_setprio(0);
      {
        const Modes M = prep_modes(prm, pdt);
        const lptr st = hs + lane * 8, rd = hs + g * 16;
        v4u Y[8], A[8];
        gen_y<528>(st, M);
        asm volatile("" ::: "memory");
        read8(Y, rd + s16 * 528);
        asm volatile("" ::: "memory");
        set_ka();
        if (h == 0) {
          gen_run<16, 528>(st, M.C, cexp2(M.al, M.wr, 1.f));
          asm volatile("" ::: "memory");
          read8(A, rd + (15 - s16) * 528);
          f32x4 k0 = dot8(A, Y, M.kscale);
          if (lane == 48) k0[3] += __uint_as_float(pds);
          put_k1<0>(ka, pack_4(k0));
          if (s16 < 15 - g) *(LAS unsigned*)(img + g * LA_KC + 16 + 2 * (2042 + 2 * g) + s16 * 4) = 0u;
        } else if (h == 1) {
          gen_run<9, 528>(st, cmul(M.C, cexp2(M.al, M.wr, 256.f)), cexp2(M.al, M.wr, 2.f));
          asm volatile("" ::: "memory");
          read8(A, rd + row_t1(s16) * 528);
          put_k1<1>(ka, pack_4(dot8(A, Y, M.kscale)));
        } else if (h == 2) {
          const cplx2 e4 = cexp2(M.al, M.wr, 4.f);
          gen_run<5, 528>(st, cmul(M.C, cexp2(M.al, M.wr, 512.f)), e4); gen_run<5, 528>(st + 5 * 528, cmul(M.C, cexp2(M.al, M.wr, 768.f)), e4);
          asm volatile("" ::: "memory");
          read8(A, rd + row_t23(tq, tp) * 528);
          const f32x4 k = dot8(A, Y, M.kscale);
          put_k2<2>(ka, pack_s4(k[0], k[1], g & 1), pack_s4(k[2], k[3], g & 1));
        } else {
          const cplx2 e8 = cexp2(M.al, M.wr, 8.f), e256 = cexp2(M.al, M.wr, 256.f);
          cplx2 b = cmul(M.C, cexp2(M.al, M.wr, 1024.f));
#pragma unroll
          for (int t = 0; t < 4; ++t) { gen_run<3, 528>(st + 3 * t * 528, b, e8); if (t < 3) b = cmul(b, e256); }
          asm volatile("" ::: "memory");
          v4u B[8];
          read8(A, rd + row_t47(tq, tp) * 528); read8(B, rd + (6 + row_t47(tq, tp)) * 528);
          const f32x4 k0 = dot8(A, Y, M.kscale), k1 = dot8(B, Y, M.kscale);
          put_k2<4>(ka, pack_s8(k0[0], k0[1]), pack_s8(k0[2], k0[3])); put_k2<6>(ka, pack_s8(k1[0], k1[1]), pack_s8(k1[2], k1[3]));
        }
      }
      STAMP(7);
      __builtin_amdgcn_s_setprio(2);
      DRAIN();
      if (lane == 0) flag_store(g_ws + WS_FC + (ab * 4 + h) * 8, tag);
      wait_tag(ws_rs, WS_FT + xcd * 512 + lane * 8, tag);
      STAMP(4);
      {
        const unsigned mo = WS_UG + (18 * xcd + aj) * 16384 + h * 4096 + lane * 16;
        const unsigned xo = WS_UG + (18 * xcd + 16 + (aj >> 3)) * 16384 + t2 * 64 + ((aj >> 1) & 3) * 16;
        v4u uv[5]; unsigned uh[5];
#pragma unroll
        for (int i = 0; i < 4; ++i) uv[i] = __builtin_amdgcn_raw_buffer_load_b128(ws_rs, mo, i * 1024, 16);
        uv[4] = __builtin_amdgcn_raw_buffer_load_b128(ws_rs, xo, 0, 16);
        uh[0] = __builtin_amdgcn_raw_buffer_load_b32(ws_rs, t2 < 4 ? WS_USED : mo - 52, 0, 16);
#pragma unroll
        for (int i = 1; i < 4; ++i) uh[i] = __builtin_amdgcn_raw_buffer_load_b32(ws_rs, mo - 52, i * 1024, 16);
        uh[4] = __builtin_amdgcn_raw_buffer_load_b32(ws_rs, t2 == 0 ? WS_USED : xo - 52, 0, 16);
        { const int ci = t2 / 28, k = t2 - ci * 28;
          *(LAS v4u*)(lds + (ci >> 1) * LA_CH + LA_U0 + (ci & 1) * LA_UC + k * 16) = (v4u){0u, 0u, 0u, 0u};
          if (t2 < 24) *(LAS v4u*)(lds + 4 * LA_CH + LA_U0 + LA_UC + (4 + t2) * 16) = (v4u){0u, 0u, 0u, 0u}; }
        SCHED();
#pragma unroll
        for (int i = 0; i < 5; ++i) {
          const lptr p = i < 4 ? lds + tp * LA_CH + LA_U0 + 448 + (64 * h + 16 * i + (lane >> 2)) * 16 : lds + 4 * LA_CH + LA_U0 + 448 + t2 * 16;
          const v4u v = uv[i];
          *(LAS v4u*)(p + LA_UC) = v;
          *(LAS v4u*)p = (v4u){__builtin_amdgcn_alignbit(v.x, uh[i], 16), __builtin_amdgcn_alignbit(v.y, v.x, 16), __builtin_amdgcn_alignbit(v.z, v.y, 16), __builtin_amdgcn_alignbit(v.w, v.z, 16)};
        }
      }
      STAMP(5);
      lds_barrier();
      STAMP(2);
      unsigned pf[2];
      { const unsigned i = aj * 256 + t2; gld32(pf[0], g_wo, i * 128u); gld32(pf[1], g_wo, (i < 1088u ? 4096u + i : 5183u) * 128u); }
      {
        kp += 128 * h; up += 128 * h;
        v4u Kf[2][8], Uf[2][8];
#pragma unroll
        for (int s = 0; s < 2; ++s)
#pragma unroll
          for (int d = 0; d < 8; ++d) { Uf[s][d] = lds16(up + 64 * s + 512 * d); Kf[s][d] = lds16(kp + 64 * s + 512 * (7 - d)); }
#pragma unroll
        for (int i = 0; i < 4; ++i) y[i] = (f32x4){0.f, 0.f, 0.f, 0.f};
        SCHED();
        auto part = [&](auto T0, auto T1, auto T2, auto T3) {
#pragma unroll
          for (int s = 0; s < 2; ++s) {
#pragma unroll
            for (int d = 0; d <= T3; ++d) y[3] = mfma_bf(Uf[s][T3 - d], Kf[s][d], y[3]);
#pragma unroll
            for (int d = 0; d <= T2; ++d) y[2] = mfma_bf(Uf[s][T2 - d], Kf[s][d], y[2]);
#pragma unroll
            for (int d = 0; d <= T1; ++d) y[1] = mfma_bf(Uf[s][T1 - d], Kf[s][d], y[1]);
#pragma unroll
            for (int d = 0; d <= T0; ++d) y[0] = mfma_bf(Uf[s][T0 - d], Kf[s][d], y[0]);
          }
        };
        if (aj & 1) part(IC(6), IC(5), IC(2), IC(1)); else part(IC(7), IC(4), IC(3), IC(0));
#pragma unroll
        for (int i = 0; i < 4; ++i) *(LAS f32x4*)(lds + LA_XP + (i * 4 + h) * 1024 + lane * 16) = y[i];
      }
      asm volatile("s_waitcnt vmcnt(0)" : "+v"(pf[0]), "+v"(pf[1]) :: "memory");
      if (w == 4) wait_tag(ws_rs, WS_FC + xcd * 512 + lane * 8, tag);
      __builtin_amdgcn_s_setprio(0);
    }
    lds_barrier();
    STAMP(3);
    {
      const lptr ob = lds + LA_OUT + tid * 4;
#pragma unroll
      for (int i = 0; i < 2; ++i) {
        const unsigned a = *(LAS unsigned*)(ob + i * 2048), b = *(LAS unsigned*)(ob + i * 2048 + 4128), c = *(LAS unsigned*)(ob + i * 2048 + 2 * 4128), d = *(LAS unsigned*)(ob + i * 2048 + 3 * 4128);
        const v4u r = {__builtin_amdgcn_perm(b, a, 0x05040100u), __builtin_amdgcn_perm(d, c, 0x05040100u), __builtin_amdgcn_perm(b, a, 0x07060302u), __builtin_amdgcn_perm(d, c, 0x07060302u)};
        __builtin_amdgcn_raw_buffer_store_b128(nz4(r), ws_rs, WS_AG + ab * 16384 + tid * 16, i * 8192, 16);
      }
      if (w >= 4) {
        const int h = w - 4;
        const lptr xp = lds + LA_XP + h * 4096 + lane * 16;
        const f32x4 v = (*(LAS f32x4*)xp + *(LAS f32x4*)(xp + 1024)) + (*(LAS f32x4*)(xp + 2048) + *(LAS f32x4*)(xp + 3072));
        const int xt = (aj & 1) ? (h == 0 ? 6 : h == 1 ? 5 : h == 2 ? 2 : 1) : (h == 0 ? 7 : h == 1 ? 4 : h == 2 ? 3 : 0);
        const unsigned o = WS_AX + (xcd * 8 + (aj >> 1)) * 4096 + xt * 512 + g * 128 + s16 * 4;
        const v2u p = pack_4(v);
        __builtin_amdgcn_raw_buffer_store_b32(nz(p.x), ws_rs, o, 0, 16); __builtin_amdgcn_raw_buffer_store_b32(nz(p.y), ws_rs, o, 64, 16);
      }
    }
    static_for<11>([&](auto I) { if (I < 10 || w == 0) wo_piece<L_WO + I * 8192>(wob, I * 8 + w, lw1k, lane); });
    {
      const unsigned mo = WS_AG + (xcd * 16 + 2 * w) * 16384 + aj * 1024 + lane * 16, xo = WS_AX + (xcd * 8 + w) * 4096 + aj * 256 + lane * 4;
      v4u f0, f1; unsigned fx;
      for (unsigned spins = 0; spins < SPIN_CAP; ++spins) {
        f0 = __builtin_amdgcn_raw_buffer_load_b128(ws_rs, mo, 0, 16); f1 = __builtin_amdgcn_raw_buffer_load_b128(ws_rs, mo, 16384, 16);
        fx = __builtin_amdgcn_raw_buffer_load_b32(ws_rs, xo, 0, 16);
        if (__all(umax4(f0, umax4(f1, fx)) != EMPTY)) break;
        __builtin_amdgcn_s_sleep(4);
        asm volatile("" ::: "memory");
      }
      const lptr fp = lds + LA_FIMG + lane * 288;
      *(LAS v4u*)(fp + w * 16) = (v4u){f0.x, f0.y, f1.x, f1.y}; *(LAS v4u*)(fp + 144 + w * 16) = (v4u){f0.z, f0.w, f1.z, f1.w};
      *(LAS u16*)(fp + 128 + w * 2) = (u16)fx; *(LAS u16*)(fp + 272 + w * 2) = (u16)(fx >> 16);
      DRAIN();
      lds_barrier();
      char* ob = g_out + (size_t)aj * 128 * 2304 + 144 * xcd;
#pragma unroll
      for (int i = 0; i < 3; ++i) {
        const unsigned c = i * 512 + tid, r = (c * 7282u) >> 16;
        if (i < 2 || w < 2) *(v4u*)(ob + r * 2160u + c * 16u) = lds16(lds + LA_FIMG + c * 16);
      }
    }
    STAMP(8);
  }

  {
    const int m = w & 3, kh = w >> 2, mt = xcd * 16 + (bi >> 3) * 4 + m;
    v4u wf[9][5];
    const lptr wb = lds + L_WO + (288 * kh + 4 * g + tq) * 144 + tp * 8;
    const lptr ws = lds + L_WO + (288 * kh + 72 * g + 16 + tq) * 144 + tp * 8;
#pragma unroll
    for (int i = 0; i < 4; ++i)
#pragma unroll
      for (int n = 0; n < 5; ++n) wf[i][n] = trd2(wb + i * 36 * 144 + n * 32, wb + (i * 36 + 20) * 144 + n * 32);
    SCHED();
    const unsigned mo = WS_OM + (mt * 16 + 8 * kh) * 1024 + lane * 16, so = WS_OS + ((xcd * 16 + 8 * kh + 2 * g) * 256 + (mt & 15) * 16 + s16) * 8;
    auto load_o = [&](v4u (&of)[9]) {
#pragma unroll
      for (int i = 0; i < 8; ++i) of[i] = __builtin_amdgcn_raw_buffer_load_b128(ws_rs, mo, i * 1024, 16);
      const v2u a = __builtin_amdgcn_raw_buffer_load_b64(ws_rs, so, 0, 16), b = __builtin_amdgcn_raw_buffer_load_b64(ws_rs, so, 2048, 16);
      of[8] = (v4u){a.x, a.y, b.x, b.y};
    };
    auto valid = [&](const v4u (&of)[9]) {
      unsigned mx = 0;
#pragma unroll
      for (int i = 0; i < 9; ++i) mx = umax4(of[i], mx);
      return (bool)__all(mx != EMPTY);
    };
    f32x4 oc[5];
    auto project = [&](const v4u (&of)[9]) {
#pragma unroll
      for (int n = 0; n < 5; ++n) oc[n] = (f32x4){0.f, 0.f, 0.f, 0.f};
#pragma unroll
      for (int i = 0; i < 9; ++i)
#pragma unroll
        for (int n = 0; n < 5; ++n) oc[n] = mfma_bf(wf[i][n], of[i], oc[n]);
    };
    v4u of[9];
    STAMP(9);
    const unsigned wwo = WS_OS + ((xcd * 16 + 8 * kh + (lane & 7)) * 256 + (mt & 15) * 16) * 8 + 124;
    unsigned spins = 0;
    if (bi >= 16 || kh) wait_word(ws_rs, wwo, spins);
    for (;;) {
      load_o(of);
      SCHED();
#pragma unroll
      for (int i = 4; i < 8; ++i)
#pragma unroll
        for (int n = 0; n < 5; ++n) wf[i][n] = trd2(wb + i * 36 * 144 + n * 32, wb + (i * 36 + 20) * 144 + n * 32);
#pragma unroll
      for (int n = 0; n < 5; ++n) wf[8][n] = trd2(ws + n * 32, ws + 36 * 144 + n * 32);
      SCHED();
      if (valid(of) || spins >= SPIN_CAP) break;
      wait_word(ws_rs, wwo, spins);
    }
    STAMP(10);
    project(of);
    if (kh) {
#pragma unroll
      for (int n = 0; n < 5; ++n) *(LAS f32x4*)(lds + L_RED + m * 5120 + n * 1024 + lane * 16) = oc[n];
    }
    lds_barrier();
    if (!kh) {
      const lptr sb = lds + L_OST + m * 16 * 176;
#pragma unroll
      for (int n = 0; n < 5; ++n) {
        const f32x4 v = oc[n] + *(LAS f32x4*)(lds + L_RED + m * 5120 + n * 1024 + lane * 16);
        *(LAS v2u*)(sb + s16 * 176 + n * 32 + g * 8) = pack_4(v);
      }
      asm volatile("" ::: "memory");
      char* ob = g_out + (size_t)mt * 16 * 2304 + 1152 + cg * 144;
#pragma unroll
      for (int j = 0; j < 3; ++j) {
        const unsigned c = j * 64 + lane, r = (c * 7282u) >> 16, k = c - r * 9u;
        if (c < 144) *(v4u*)(ob + r * 2304u + k * 16u) = lds16(sb + r * 176 + k * 16);
      }
    }
    if (blk == 0 && tid == 256) flag_store(g_ws + WS_EPOCH, tag);
  }
}

extern "C" void kernel_launch(void* const* d_in, const int* in_sizes, int n_in, void* d_out, int out_size, void* d_ws, size_t ws_size,
                              hipStream_t stream) {
  (void)in_sizes; (void)n_in; (void)out_size; (void)ws_size;
  (void)hipFuncSetAttribute((const void*)fused_kernel, hipFuncAttributeMaxDynamicSharedMemorySize, LDS_BYTES);
  fused_kernel<<<256, 512, LDS_BYTES, stream>>>((const char*)d_in[0], (const char*)d_in[1], (const char*)d_in[2], (const char*)d_in[3],
                                                (const char*)d_in[4], (const char*)d_in[5], (const char*)d_in[6], (const char*)d_in[7],
                                                (const char*)d_in[8], (const char*)d_in[9], (const char*)d_in[10], (char*)d_out, (char*)d_ws);
}
```

```cpp
#include <hip/hip_runtime.h>
#include <type_traits>

typedef unsigned short u16;
typedef unsigned long long u64;
typedef unsigned v4u __attribute__((ext_vector_type(4)));
typedef unsigned v3u __attribute__((ext_vector_type(3)));
typedef unsigned v2u __attribute__((ext_vector_type(2)));
typedef float f32x4 __attribute__((ext_vector_type(4)));
typedef float f32x2 __attribute__((ext_vector_type(2)));
typedef short s16x4 __attribute__((ext_vector_type(4)));
typedef __bf16 bf16x8 __attribute__((ext_vector_type(8)));
typedef __bf16 bf16x2 __attribute__((ext_vector_type(2)));
typedef _Float16 f16x8 __attribute__((ext_vector_type(8)));
typedef _Float16 f16x2 __attribute__((ext_vector_type(2)));
#define LAS __attribute__((address_space(3)))
#define GAS __attribute__((address_space(1)))
#define DI __device__ __forceinline__
typedef LAS char* lptr;

constexpr int WS_EPOCH = 0;
constexpr int WS_FT = 851968;
constexpr int WS_FO = 917504;
constexpr int WS_FA = 933888;
constexpr int WS_FH = 950272;
constexpr int WS_PART = 16384;
constexpr int WS_OM = 1 << 20;
constexpr int WS_OS = 3 << 20;
constexpr int WS_UG = 4 << 20;
constexpr int WS_AX = 9 << 20;
constexpr int WS_DBG = 10 << 20;
constexpr int WS_USED = 11 << 20;
constexpr int LDS_BYTES = 160 * 1024;
constexpr int CODE_BYTES = 48640;
constexpr unsigned EMPTY = 0xffffffffu;

constexpr int L_WO = 0, LB_IMG = 92160, LB_WF = 149504, LB_KVF = 158720, LB_WPHI = LB_KVF;
constexpr int wslot(int t) { return (t % 3) * 49152 + 32768; }
constexpr int xslot(int t) { return (t % 3) * 49152; }
constexpr int KV_EM = 2048, KV_NX = 3072, KV_EX = 3584, KV_END = 3712, KV_GO = 3840;
constexpr int L_RED = 112640, L_OST = L_RED + 20480;
constexpr int LA_KC = 4160, LA_UC = 4640, LA_U0 = 4 * LA_KC, LA_CH = 4 * LA_KC + 2 * LA_UC;
constexpr int LA_HS = 5 * LA_CH, LA_HSW = 8448;
constexpr int LA_SYNC = LA_HS + 4 * LA_HSW;
constexpr int LA_OUT = LA_HS, LA_XP = LA_OUT + 4 * 4128, LA_FIMG = 92160;

#define STAMP_INS "s_memrealtime"
#define PROFW 0
#define STAMP_(i) do {} while (0)
#define STAMP(i) do {} while (0)

DI void stamp_fn(u64& t) { asm volatile(STAMP_INS " %0\n\ts_waitcnt lgkmcnt(0)" : "=s"(t) :: "memory"); }
DI float bf2f(unsigned lo16) { return __uint_as_float(lo16 << 16); }
DI unsigned pk_bf16(float a, float b) { f32x2 v = {a, b}; return __builtin_bit_cast(unsigned, __builtin_convertvector(v, bf16x2)); }
DI unsigned pk_f16(float a, float b) { f32x2 v = {a, b}; return __builtin_bit_cast(unsigned, __builtin_convertvector(v, f16x2)); }
DI float rnd_bf16(float a) { return bf2f(pk_bf16(a, a) & 0xffffu); }
DI f32x4 mfma_bf(v4u a, v4u b, f32x4 c) { return __builtin_amdgcn_mfma_f32_16x16x32_bf16(__builtin_bit_cast(bf16x8, a), __builtin_bit_cast(bf16x8, b), c, 0, 0, 0); }
DI void mfma_acc(f32x4& c, v4u a, v4u b) { asm volatile("v_mfma_f32_16x16x32_bf16 %0, %1, %2, %0" : "+v"(c) : "v"(a), "v"(b)); }
DI void mfma_new(f32x4& c, v4u a, v4u b) { asm volatile("v_mfma_f32_16x16x32_bf16 %0, %1, %2, 0" : "=&v"(c) : "v"(a), "v"(b)); }
#define MFMA_FENCE() do { __builtin_amdgcn_sched_barrier(0); asm volatile("s_nop 7" ::: "memory"); __builtin_amdgcn_sched_barrier(0); } while (0)
DI f32x4 mfma_h(v4u a, v4u b, f32x4 c) { return __builtin_amdgcn_mfma_f32_16x16x32_f16(__builtin_bit_cast(f16x8, a), __builtin_bit_cast(f16x8, b), c, 0, 0, 0); }
DI v2u trd(lptr p) { return __builtin_bit_cast(v2u, __builtin_amdgcn_ds_read_tr16_b64_v4i16((LAS s16x4*)p)); }
DI v4u trd2(lptr p0, lptr p1) { v2u a = trd(p0), b = trd(p1); return (v4u){a.x, a.y, b.x, b.y}; }
DI v4u lds16(lptr p) { return *(LAS v4u*)p; }
template <int IMM, bool NT = false> DI void glds(const char* base, unsigned voff, unsigned lbase) {
  if constexpr (NT) asm volatile("s_add_u32 m0, %2, %3\n\ts_nop 0\n\tglobal_load_lds_dwordx4 %0, %1 nt" :: "v"(voff), "s"(base), "s"(lbase), "i"(IMM) : "memory", "scc");
  else asm volatile("s_add_u32 m0, %2, %3\n\ts_nop 0\n\tglobal_load_lds_dwordx4 %0, %1" :: "v"(voff), "s"(base), "s"(lbase), "i"(IMM) : "memory", "scc");
}
template <int IMM, bool NT = false> DI void glds4(const char* base, unsigned voff, unsigned lbase) {
  if constexpr (NT) asm volatile("s_add_u32 m0, %2, %3\n\ts_nop 0\n\tglobal_load_lds_dword %0, %1 nt" :: "v"(voff), "s"(base), "s"(lbase), "i"(IMM) : "memory", "scc");
  else asm volatile("s_add_u32 m0, %2, %3\n\ts_nop 0\n\tglobal_load_lds_dword %0, %1" :: "v"(voff), "s"(base), "s"(lbase), "i"(IMM) : "memory", "scc");
}
#define IC(n) std::integral_constant<int, (n)>{}
template <int N, int I = 0, class F> DI void static_for(F&& f) { if constexpr (I < N) { f(std::integral_constant<int, I>{}); static_for<N, I + 1>(f); } }
DI void gld32(unsigned& d, const char* base, unsigned voff) { asm volatile("global_load_dword %0, %1, %2" : "=v"(d) : "v"(voff), "s"(base) : "memory"); }
DI void gld32p(unsigned& d, const char* p) { asm volatile("global_load_dword %0, %1, off" : "=v"(d) : "v"(p) : "memory"); }
DI void gld16(unsigned& d, const char* base, unsigned voff) { asm volatile("global_load_ushort %0, %1, %2" : "=v"(d) : "v"(voff), "s"(base) : "memory"); }
DI void gld96(v3u& d, const char* base, unsigned voff) { asm volatile("global_load_dwordx3 %0, %1, %2" : "=v"(d) : "v"(voff), "s"(base) : "memory"); }
#define WAITCNT(vm, lgkm) __builtin_amdgcn_s_waitcnt(((vm) & 15) | (((vm) >> 4) << 14) | 0x70 | ((lgkm) << 8))
#define BAR() __builtin_amdgcn_s_barrier()
#define SCHED() __builtin_amdgcn_sched_barrier(0)
#define DRAIN() asm volatile("s_waitcnt vmcnt(0)" ::: "memory")
DI void lds_barrier() { asm volatile("" ::: "memory"); WAITCNT(63, 0); BAR(); asm volatile("" ::: "memory"); }
DI f32x4 sel4(bool c, f32x4 a, f32x4 b) { return (f32x4){c ? a[0] : b[0], c ? a[1] : b[1], c ? a[2] : b[2], c ? a[3] : b[3]}; }
DI void ds_write2(unsigned addr, unsigned a, unsigned b) { asm volatile("ds_write2_b32 %0, %1, %2 offset1:1" :: "v"(addr), "v"(a), "v"(b) : "memory"); }

DI u64 mix64(u64 z) { z += 0x9E3779B97F4A7C15ull; z = (z ^ (z >> 30)) * 0xBF58476D1CE4E5B9ull; z = (z ^ (z >> 27)) * 0x94D049BB133111EBull; return z ^ (z >> 31); }
DI u64 read_tag(const char* ws) {
  u64 e; asm volatile("s_load_dwordx2 %0, %1, 0x0\n\ts_waitcnt lgkmcnt(0)" : "=s"(e) : "s"(ws) : "memory");
  return mix64(e);
}
#define FLAG_SCOPE __HIP_MEMORY_SCOPE_AGENT
DI void flag_store(char* p, u64 tag) { __hip_atomic_store((GAS u64*)p, tag, __ATOMIC_RELAXED, FLAG_SCOPE); }
constexpr unsigned SPIN_CAP = 1u << 20;
#define POLL_AUX 16
#define OUTA_AUX 0
#define OUTB_AUX 0
#define CONV_IT 4
#define P3_PRE 0
#define J1_AT 0
#define PF0 0
#define WV_PRE 4
#define FO_SLEEP 1
#define T_GAP 0
#define EARLY_SETUP 1
#define LPRIO 0
#define WOPF_AT 0
#define F_AT 2
#define J2_AT 2
#define HC_PRE 3
#define WO7 6
#define WO8 5
#define AG_GAP 0
#define FH_ON 1
#define FIMG_EARLY 1
#define FR_AT 6
#define OUTC_AUX 0
#define J1_PRE 4
#define T_AT 1
#define NT_U 1
#define NT_P 0
#define FT_PRE 1
#define FT_SLEEP 3
#define O_SLEEP 5
#define WW_SLEEP 8
#define HOP1_SLEEP 4
template <int SLEEP = 8> DI bool wait_tag(__amdgpu_buffer_rsrc_t rs, unsigned off, u64 tag) {
  unsigned spins = 0;
#pragma unroll 1
  for (; spins < SPIN_CAP; ++spins) {
    const v2u v = __builtin_amdgcn_raw_buffer_load_b64(rs, off, 0, POLL_AUX);
    if (__all(v.x == (unsigned)tag && v.y == (unsigned)(tag >> 32))) break;
    __builtin_amdgcn_s_sleep(SLEEP);
    asm volatile("" ::: "memory");
  }
  asm volatile("" ::: "memory");
  return spins != 0;
}
DI void wait_word(__amdgpu_buffer_rsrc_t rs, unsigned off, unsigned& spins) {
#pragma unroll 1
  for (; spins < SPIN_CAP; ++spins) {
    if (__all(__builtin_amdgcn_raw_buffer_load_b32(rs, off, 0, POLL_AUX) != EMPTY)) break;
    __builtin_amdgcn_s_sleep(WW_SLEEP);
    asm volatile("" ::: "memory");
  }
  ++spins;
  asm volatile("" ::: "memory");
}
DI void load12(v4u& a0, v4u& a1, v4u& a2, v4u& a3, v4u& a4, v4u& a5, v4u& a6, v4u& a7, v4u& b0, v4u& b1, v4u& b2, v4u& b3,
               unsigned off, unsigned off2, const char* base, unsigned have) {
#define L8(i, a, o) "s_bitcmp1_b32 %15, " #i "\n\ts_cbranch_scc1 .Lskip" #i "_%=\n\tglobal_load_dwordx4 %" #i ", %" #a ", %14 offset:" #o " sc1\n.Lskip" #i "_%=:\n\t"
  asm volatile(L8(0, 12, -4096) L8(1, 12, -3072) L8(2, 12, -2048) L8(3, 12, -1024) L8(4, 12, 0) L8(5, 12, 1024) L8(6, 12, 2048) L8(7, 12, 3072)
               L8(8, 13, 0) L8(9, 13, 1024) L8(10, 13, 2048) L8(11, 13, 3072) "s_waitcnt vmcnt(0)"
               : "+v"(a0), "+v"(a1), "+v"(a2), "+v"(a3), "+v"(a4), "+v"(a5), "+v"(a6), "+v"(a7), "+v"(b0), "+v"(b1), "+v"(b2), "+v"(b3)
               : "v"(off), "v"(off2), "s"(base), "s"(have) : "memory", "scc");
#undef L8
}
DI unsigned umax3(unsigned a, unsigned b, unsigned c) { return max(max(a, b), c); }
DI unsigned umax4(const v4u& v, unsigned m) { return umax3(umax3(v.x, v.y, v.z), v.w, m); }
DI unsigned nz(unsigned v) { return min(v, EMPTY - 1u); }
DI v4u nz4(v4u v) { return (v4u){nz(v.x), nz(v.y), nz(v.z), nz(v.w)}; }

DI void swap32(float& a, float& b) { auto r = __builtin_amdgcn_permlane32_swap(__float_as_uint(a), __float_as_uint(b), false, false); a = __uint_as_float(r[0]); b = __uint_as_float(r[1]); }
DI void swap16(float& a, float& b) { auto r = __builtin_amdgcn_permlane16_swap(__float_as_uint(a), __float_as_uint(b), false, false); a = __uint_as_float(r[0]); b = __uint_as_float(r[1]); }
DI float mx3(float a, float b, float c) { return __builtin_elementwise_maximum(__builtin_elementwise_maximum(a, b), c); }
DI float mn3(float a, float b, float c) { return __builtin_elementwise_minimum(__builtin_elementwise_minimum(a, b), c); }
DI f32x4 mfma_bf16k(v2u a, v2u b, f32x4 c) { return mfma_bf((v4u){a.x, a.y, 0u, 0u}, (v4u){b.x, b.y, 0u, 0u}, c); }

template <class T> DI void pin(T& v) { asm volatile("" : "+v"(v)); }
DI void row_range(const f32x4 (&F)[3], int which, float (&mn)[2]) {
  if (which == 0) mn[0] = mx3(mx3(F[0][0], F[0][1], F[0][2]), mx3(F[0][3], F[1][0], F[1][1]), mx3(F[1][2], F[1][3], F[2][0]));
  else mn[1] = -mn3(mn3(F[0][0], F[0][1], F[0][2]), mn3(F[0][3], F[1][0], F[1][1]), mn3(F[1][2], F[1][3], F[2][0]));
}
DI void features2(const f32x4 (&Fa)[3], const f32x4 (&Fb)[3], const float (&MN)[2][2], unsigned (&Pa)[9], unsigned (&Pb)[9]) {
  const float L = 1.4426950408889634f;
  const float F[2][9] = {{Fa[0][0], Fa[0][1], Fa[0][2], Fa[0][3], Fa[1][0], Fa[1][1], Fa[1][2], Fa[1][3], Fa[2][0]},
                         {Fb[0][0], Fb[0][1], Fb[0][2], Fb[0][3], Fb[1][0], Fb[1][1], Fb[1][2], Fb[1][3], Fb[2][0]}};
  float m[2], n[2], t1[2], t2[2];
#pragma unroll
  for (int k = 0; k < 2; ++k) { m[k] = MN[k][0]; n[k] = MN[k][1]; }
#pragma unroll
  for (int k = 0; k < 2; ++k) swap16(m[k], n[k]);
#pragma unroll
  for (int k = 0; k < 2; ++k) { t1[k] = __builtin_elementwise_maximum(m[k], n[k]) * -L; t2[k] = t1[k]; }
#pragma unroll
  for (int k = 0; k < 2; ++k) swap16(t1[k], t2[k]);
  const f32x2 LL = {L, -L};
  f32x2 e[2][9], sm[2];
#pragma unroll
  for (int k = 0; k < 2; ++k) {
    const f32x2 off = {t1[k], t2[k]};
#pragma unroll
    for (int j = 0; j < 9; ++j) {
      const f32x2 x = __builtin_elementwise_fma((f32x2){F[k][j], F[k][j]}, LL, off);
      e[k][j] = (f32x2){__builtin_amdgcn_exp2f(x[0]), __builtin_amdgcn_exp2f(x[1])};
    }
  }
#pragma unroll
  for (int k = 0; k < 2; ++k) { sm[k] = ((e[k][0] + e[k][1]) + (e[k][2] + e[k][3])) + ((e[k][4] + e[k][5]) + (e[k][6] + e[k][7])) + e[k][8]; m[k] = sm[k][0]; n[k] = sm[k][1]; }
#pragma unroll
  for (int k = 0; k < 2; ++k) swap16(m[k], n[k]);
#pragma unroll
  for (int k = 0; k < 2; ++k) { t1[k] = __builtin_amdgcn_rcpf(m[k] + n[k]); t2[k] = t1[k]; }
#pragma unroll
  for (int k = 0; k < 2; ++k) swap16(t1[k], t2[k]);
#pragma unroll
  for (int j = 0; j < 9; ++j) { const f32x2 v = e[0][j] * (f32x2){t1[0], t2[0]}; Pa[j] = pk_bf16(v[0], v[1]); }
#pragma unroll
  for (int j = 0; j < 9; ++j) { const f32x2 v = e[1][j] * (f32x2){t1[1], t2[1]}; Pb[j] = pk_bf16(v[0], v[1]); }
}

struct cplx2 { f32x2 r, i; };
DI cplx2 cmul(const cplx2& a, const cplx2& b) { cplx2 c; c.r = a.r * b.r - a.i * b.i; c.i = a.r * b.i + a.i * b.r; return c; }
DI cplx2 cexp2(f32x2 al, f32x2 wr, float p) {
  cplx2 c;
#pragma unroll
  for (int k = 0; k < 2; ++k) {
    float m = __builtin_amdgcn_exp2f(al[k] * p), f = __builtin_amdgcn_fractf(wr[k] * p);
    c.r[k] = m * __builtin_amdgcn_cosf(f); c.i[k] = m * __builtin_amdgcn_sinf(f);
  }
  return c;
}
DI void put_row(lptr p, const cplx2& v) { *(LAS v2u*)p = (v2u){pk_f16(v.r[0], v.i[0]), pk_f16(v.r[1], v.i[1])}; }

DI float exp_acc(float x) {
  const float e = __builtin_amdgcn_exp2f(x * 1.44268798828125f);
  return fmaf(e, x * 4.888496058e-06f, e);
}
template <int CTRL> DI unsigned dpp_umax(unsigned v) { return max(v, (unsigned)__builtin_amdgcn_update_dpp(0u, v, CTRL, 0xf, 0xf, true)); }

struct Modes { f32x2 al, wr; cplx2 C; float kscale; };
DI Modes prep_modes(const unsigned (&q)[4], unsigned ldt) {
  Modes M;
  const float dt = rnd_bf16(exp_acc(__uint_as_float(ldt)));
  const unsigned ar = pk_bf16(exp_acc(bf2f(q[0])), exp_acc(__uint_as_float(q[0] & 0xffff0000u)));
  M.al = (f32x2){bf2f(ar), __uint_as_float(ar & 0xffff0000u)} * dt * -1.4426950408889634f;
  M.wr = (f32x2){bf2f(q[1]), __uint_as_float(q[1] & 0xffff0000u)} * dt * 0.15915494309189535f;
  M.C.r = (f32x2){bf2f(q[2]), __uint_as_float(q[2] & 0xffff0000u)}; M.C.i = (f32x2){bf2f(q[3]), __uint_as_float(q[3] & 0xffff0000u)};
  unsigned mx = max(max((q[2] << 16) & 0x7fffffffu, q[2] & 0x7fffffffu), max((q[3] << 16) & 0x7fffffffu, q[3] & 0x7fffffffu));
  mx = dpp_umax<0x111>(mx); mx = dpp_umax<0x112>(mx); mx = dpp_umax<0x114>(mx); mx = dpp_umax<0x118>(mx); mx = dpp_umax<0x142>(mx); mx = dpp_umax<0x143>(mx);
  int ex = __builtin_amdgcn_readlane(mx, 63) >> 23; ex = ex < 30 ? 30 : (ex > 230 ? 230 : ex);
  const float sc = __uint_as_float((unsigned)(267 - ex) << 23);
  M.kscale = 2.f * dt * __uint_as_float((unsigned)(ex - 28) << 23);
  M.C.r *= sc; M.C.i *= sc;
  return M;
}
template <int N, int S> DI void gen_run(lptr p, cplx2 x, const cplx2& step) {
#pragma unroll
  for (int r = 0; r < N; ++r) { put_row(p + r * S, x); if (r + 1 < N) x = cmul(x, step); }
}
template <int S> DI void gen_y(lptr p, const Modes& M) {
  cplx2 e = cexp2(M.al, M.wr, 16.f); e.i = -e.i;
  cplx2 y; y.r = (f32x2){32768.f, 32768.f}; y.i = (f32x2){0.f, 0.f};
  put_row(p, y); y.r = e.r * 32768.f; y.i = e.i * 32768.f;
  gen_run<15, S>(p + S, y, e);
}
DI void read8(v4u (&f)[8], lptr p) {
#pragma unroll
  for (int ks = 0; ks < 8; ++ks) f[ks] = lds16(p + ks * 64);
}
DI f32x4 dot8(const v4u (&a)[8], const v4u (&y)[8], float sc) {
  f32x4 k = {0.f, 0.f, 0.f, 0.f};
#pragma unroll
  for (int ks = 0; ks < 8; ++ks) k = mfma_h(a[ks], y[ks], k);
  return k * sc;
}
DI int row_t1(int s16) { const int r = 15 - s16; return (r + ((r >> 1) & 1)) >> 1; }
DI int row_t23(int tq, int tp) { return 5 * (tp >> 1) + 4 - tq - (tp & 1); }
DI int row_t47(int tq, int tp) { const int rg = 3 - tq; return 3 * (tp >> 1) + ((tp & 1) ? (rg == 3 ? 2 : rg >> 1) : (rg + 1) >> 1); }
DI v2u pack_4(const f32x4& k) { return (v2u){pk_bf16(k[0], k[1]), pk_bf16(k[2], k[3])}; }
DI v2u pack_s4(float a, float b, bool odd) { return (v2u){pk_bf16(a, odd ? b : a), pk_bf16(b, b)}; }
DI v2u pack_s8(float a, float b) { return (v2u){pk_bf16(a, a), pk_bf16(a, b)}; }
template <int T> DI void put_k2(const unsigned (&ka)[8], const v2u& a, const v2u& b) {
#pragma unroll
  for (int k = 0; k < 4; ++k)
    asm volatile("ds_write2st64_b32 %0, %2, %4 offset0:%6 offset1:%7\n\tds_write2st64_b32 %1, %3, %5 offset0:%6 offset1:%7"
                 :: "v"(ka[k]), "v"(ka[4 + k]), "v"(a.x), "v"(a.y), "v"(b.x), "v"(b.y), "i"(2 * (7 - T)), "i"(2 * (6 - T)) : "memory");
}
template <int T> DI void put_k1(const unsigned (&ka)[8], const v2u& a) {
#pragma unroll
  for (int k = 0; k < 4; ++k) asm volatile("ds_write_b32 %0, %1 offset:%3\n\tds_write_b32 %0, %2 offset:%4" :: "v"(ka[k]), "v"(a.x), "v"(a.y), "i"(512 * (7 - T)), "i"(512 * (7 - T) + 4) : "memory");
}

__global__ __launch_bounds__(512, 2) void fused_kernel(
    const char* __restrict__ g_u, const char* __restrict__ g_lar, const char* __restrict__ g_aim, const char* __restrict__ g_cr,
    const char* __restrict__ g_ci, const char* __restrict__ g_ldt, const char* __restrict__ g_dsk, const char* __restrict__ g_x,
    const char* __restrict__ g_w, const char* __restrict__ g_wphi, const char* __restrict__ g_wo, char* __restrict__ g_out, char* g_ws) {
  extern __shared__ __attribute__((aligned(16))) char smem[];
  const lptr lds = (lptr)smem;
  const int tid = threadIdx.x, lane = tid & 63, w = __builtin_amdgcn_readfirstlane(tid >> 6) & 7;
  const int blk = blockIdx.x, xcd = blk & 7, bi = blk >> 3;
  const int g = lane >> 4, s16 = lane & 15, tq = (lane >> 2) & 3, tp = lane & 3;
  const int pc = (bi & 3) * (160 - (bi >> 4) * 32) + 512 - (bi >> 4) * 512;
  const unsigned lw1k = (unsigned)(size_t)lds + w * 1024;
  STAMP(0);
  unsigned cpf;
  { const char* code0; asm volatile("s_getpc_b64 %0" : "=s"(code0));
    gld32(cpf, code0, min((unsigned)(bi * 16 + (tid >> 5)) * 128u, (unsigned)CODE_BYTES - 128u));
  }
  SCHED();
  asm volatile("" :: "s"(g_u), "s"(g_lar), "s"(g_aim), "s"(g_cr), "s"(g_ci), "s"(g_ldt), "s"(g_dsk), "s"(g_x), "s"(g_w), "s"(g_wphi), "s"(g_wo), "s"(g_out), "s"(g_ws));
  const int rb = (xcd >> 1) * 2 + (bi >> 3), hp = (xcd & 1) * 8 + (bi & 7);
  const int hq = hp ^ 8;
  const int aj = bi - 16, ab = xcd * 16 + aj;
  const int xo2 = xcd ^ 2;
  const int tsw = (aj & 7) == xcd, tg_ = (aj & 7) ^ tsw, tr = 2 * (xcd ^ tsw) + (aj >> 3);
  const int lw = w & 3, lr = lane >> 3, lc = lane & 7;
  unsigned w_off[4];
  unsigned x_off0 = lr * 1152u + ((lc ^ g) << 4), x_off1 = (8 + lr) * 1152u + ((lc ^ (4 + g)) << 4);
  const char* xb0 = g_x + (size_t)(rb * 256 + lw * 64) * 1152;
  const char *xb1 = xb0 + 16 * 1152, *xb2 = xb0 + 32 * 1152, *xb3 = xb0 + 48 * 1152;
  const unsigned l1k = (unsigned)(size_t)lds + lw * 1024, l8k = (unsigned)(size_t)lds + lw * 8192;
  const int ch = 72 * xcd + (w < 4 ? 4 * aj + w : 64 + (aj >> 1));
  const unsigned lprm = (unsigned)(size_t)lds + (w < 4 ? w * LA_CH : LA_HS + lw * LA_HSW);
  const char* pfb = aj < 6 ? g_x + (xcd >> 1) * 512 * 1152 : g_w;
  auto prefetch = [&](auto J) {
    int td = lane; asm volatile("" : "+v"(td));
    unsigned o;
    if (aj < 6) { const unsigned i = min((J * 4u + lw) * 384u + aj * 64 + td, 4607u); o = (i & 511u) * 1152u + (i >> 9) * 128u; }
    else { const unsigned i = min((J * 4u + lw) * 640u + (aj - 6) * 64 + td, 8639u), r = i * 34953u >> 19, q = i - r * 15u; o = r * 3456u + ((q * 13u >> 6) + ((xcd ^ (q < 5u)) & 1)) * 512u + q * 128u; }
    if (NT_P == 2 || (NT_P == 1 && aj < 6)) glds4<0, true>(pfb, o, (unsigned)(size_t)lds + LDS_BYTES - 256);
    else glds4<0>(pfb, o, (unsigned)(size_t)lds + LDS_BYTES - 256);
  };
  if (bi < 16) {
    if (w >= 4) {
      constexpr int o = xslot(0), p = wslot(0);
      glds<o>(xb0, x_off0, l8k); glds<o + 1024>(xb0, x_off1, l8k); glds<o + 2048>(xb1, x_off0, l8k); glds<o + 3072>(xb1, x_off1, l8k);
#pragma unroll
      for (int i = 0; i < 4; ++i) {
        const unsigned q = min(4 * i + lw, 14), part = q * 13u >> 6, c = (q - part * 5u) * 64u + lane, pi = c * 13108u >> 16;
        w_off[i] = ((pi & 56u) + ((pi & 1u) << 2) + ((pi >> 1) & 3u)) * 3456u + (c - pi * 5u) * 16u + (part * 1152u + ((72 * (part ? hp : hq)) & ~15));
      }
      glds<p>(g_w, w_off[0], l1k); glds<p + 4096>(g_w, w_off[1], l1k); glds<p + 8192>(g_w, w_off[2], l1k); if (lw < 3) glds<p + 12288>(g_w, w_off[3], l1k);
      glds<o + 4096>(xb2, x_off0, l8k); glds<o + 5120>(xb2, x_off1, l8k); glds<o + 6144>(xb3, x_off0, l8k); glds<o + 7168>(xb3, x_off1, l8k);
    } else glds<LB_WPHI>(g_wphi + (w < 2 ? hp : hq) * 1296, min(tid & 127, 80) * 16, lw1k);
  } else {
    const unsigned o = ch * 256 + lane * 4;
    if (w < 4) prefetch(IC(0));
    glds4<0, NT_U>(g_lar, o, lprm); glds4<256, NT_U>(g_aim, o, lprm); glds4<512, NT_U>(g_cr, o, lprm); glds4<768, NT_U>(g_ci, o, lprm);
    if (w >= 4) {
      if (J1_PRE) __builtin_amdgcn_s_sleep(J1_PRE);
      if (J1_AT == 0 || (J1_AT == 2 && w < 6)) prefetch(IC(1));
      const unsigned lh = (unsigned)(size_t)lds + lw * LA_CH;
      const char* ubase = g_u + (size_t)(128 * tr + 32 * lw) * 1152 + 144 * tg_;
      static_for<5>([&](auto I) { const unsigned c0 = I * 64 + lane, c = c0 < 288u ? c0 : 287u, r = (c * 7282u) >> 16; glds<LA_U0 + I * 1024, NT_U>(ubase, r * 1008u + c * 16u, lh); });
    }
    if ((w >= 4 && J1_AT == 1) || (w >= 6 && J1_AT == 2)) prefetch(IC(1));
  }
  asm volatile("" ::: "memory");
  const __amdgpu_buffer_rsrc_t ws_rs = __builtin_amdgcn_make_buffer_rsrc(g_ws, 0, WS_USED, 0x00020000);
  const char* wob = g_wo + pc;
  u64 tag = 0;
  int role = ((bi >> 4) << 1) | ((w >> 2) ^ 1);
  asm volatile("" : "+s"(role));

  auto project = [&](auto NT_) {
    constexpr int NT = NT_; constexpr bool ssm = NT == 4;
    constexpr int RS = NT * 32 + 16, NS = ssm ? 2 : 3;
    const int m = w & 3, kh = w >> 2, mt = xo2 * 16 + ((bi >> 2) & 3) * 4 + m;
    const __amdgpu_buffer_rsrc_t out_rs = __builtin_amdgcn_make_buffer_rsrc(g_out, 0, 2048 * 2304, 0x00020000);
    v4u wf[9][NT];
    const lptr wb = lds + L_WO + (288 * kh + 4 * g + tq) * 160 + tp * 8, ws = lds + L_WO + (288 * kh + 72 * g + 16 + tq) * 160 + tp * 8;
#pragma unroll
    for (int i = 0; i < 8; ++i) {
      if (ssm && i == 2) { SCHED(); DRAIN(); if (lane == 0) flag_store(g_ws + WS_FA + (ab * 8 + w) * 8, tag); SCHED(); }
#pragma unroll
      for (int n = 0; n < NT; ++n) wf[i][n] = trd2(wb + i * 36 * 160 + n * 32, wb + (i * 36 + 20) * 160 + n * 32);
    }
#pragma unroll
    for (int n = 0; n < NT; ++n) wf[8][n] = trd2(ws + n * 32, ws + 36 * 160 + n * 32);
    const unsigned mo = WS_OM + (mt * 16 + 8 * kh) * 1024 + lane * 16, so = WS_OS + ((xo2 * 16 + 8 * kh + 2 * g) * 256 + (mt & 15) * 16 + s16) * 8;
    unsigned spins = 0;
    lptr rdp, stw, str[NS]; unsigned sto[NS];
    WAITCNT(63, 0);
    STAMP_(9);
    if (w == (ssm ? 0 : 7)) { if (wait_tag<FO_SLEEP>(ws_rs, WS_FO + xo2 * 128 + s16 * 8, tag)) __builtin_amdgcn_s_sleep(O_SLEEP); }
    BAR(); asm volatile("" ::: "memory");
    const int fg = aj & 7, fr = 2 * xcd + (aj >> 3);
    v4u fq[4]; v2u fx; bool fgot = false;
    auto fetch_rows = [&](bool on, int ln) {
      const unsigned fo = on ? WS_UG + (fg * 18 + (ln >> 4)) * 16384 + fr * 1024 + w * 256 + (ln & 15) * 16 : WS_USED;
#pragma unroll
      for (int i = 0; i < 4; ++i) fq[i] = __builtin_amdgcn_raw_buffer_load_b128(ws_rs, fo + i * 65536, 0, 16);
      fx = __builtin_amdgcn_raw_buffer_load_b64(ws_rs, on ? WS_AX + (fg * 8 + (ln >> 3)) * 4096 + fr * 256 + w * 64 + (ln & 7) * 8 : WS_USED, 0, 16);
    };
    auto stage_rows = [&]() {
        const lptr fb = lds + LA_FIMG + w * 4608, fp = fb + s16 * 288 + g * 8, fe = fb + (lane & 7) * 576 + 128 + (lane >> 3) * 2;
#pragma unroll
        for (int i = 0; i < 4; ++i) { *(LAS v2u*)(fp + i * 32) = (v2u){fq[i].x, fq[i].y}; *(LAS v2u*)(fp + i * 32 + 144) = (v2u){fq[i].z, fq[i].w}; }
        *(LAS u16*)(fe) = (u16)fx.x; *(LAS u16*)(fe + 144) = (u16)(fx.x >> 16); *(LAS u16*)(fe + 288) = (u16)fx.y; *(LAS u16*)(fe + 432) = (u16)(fx.y >> 16);
    };
    f32x4 oc[NT];
    for (;;) {
      v4u of[9], fav;
      int ln = lane; asm volatile("" : "+v"(ln));
      if constexpr (ssm) { fav = __builtin_amdgcn_raw_buffer_load_b128(ws_rs, (kh ? WS_USED : WS_FA + fg * 1024) + ln * 16, 0, 16); SCHED(); }
#pragma unroll
      for (int i = 0; i < 8; ++i) { of[i] = __builtin_amdgcn_raw_buffer_load_b128(ws_rs, mo, i * 1024, 16); SCHED(); }
      const v2u a = __builtin_amdgcn_raw_buffer_load_b64(ws_rs, so, 0, 16), b = __builtin_amdgcn_raw_buffer_load_b64(ws_rs, so, 2048, 16);
      of[8] = (v4u){a.x, a.y, b.x, b.y};
      SCHED();
      {
        rdp = lds + L_RED + m * 5120 + ln * 16; stw = lds + L_OST + m * 16 * RS + (ln & 15) * RS + (ln >> 4) * 8;
#pragma unroll
        for (int j = 0; j < NS; ++j) {
          const unsigned c = min(j * 64 + ln, NT * 32 - 1), r = ssm ? c >> 3 : (c * 6554u) >> 16;
          str[j] = lds + L_OST + m * 16 * RS + r * 16 + c * 16;
          sto[j] = (j < 2 || ln < 32) ? mt * 16 * 2304 + 1152 + pc + r * (2304u - NT * 32u) + c * 16u : 2048u * 2304u;
          pin(str[j]); pin(sto[j]);
        }
        pin(rdp); pin(stw);
      }
      unsigned mx = 0;
#pragma unroll
      for (int n = 0; n < NT; ++n) oc[n] = (f32x4){0.f, 0.f, 0.f, 0.f};
#pragma unroll
      for (int i = 0; i < 9; ++i) {
        SCHED();
#pragma unroll
        for (int n = 0; n < NT; ++n) oc[n] = mfma_bf(wf[i][n], of[i], oc[n]);
        mx = umax4(of[i], mx);
        if (ssm && i == FR_AT) {
          fgot = __all(fav.x == (unsigned)tag && fav.z == (unsigned)tag && fav.y == (unsigned)(tag >> 32) && fav.w == (unsigned)(tag >> 32));
          fetch_rows(fgot, ln);
        }
      }
      if (__all(mx != EMPTY) || spins >= SPIN_CAP) break;
      const unsigned wwo = WS_OS + ((xo2 * 16 + 8 * kh + (ln & 7)) * 256 + (mt & 15) * 16) * 8 + 124;
      wait_word(ws_rs, wwo, spins);
    }
    STAMP_(10);
    volatile LAS unsigned* pair = (volatile LAS unsigned*)(lds + (ssm ? LA_SYNC + 32 : LB_KVF + KV_GO + 32) + m * 4);
    if (!kh) {
#pragma unroll
      for (int n = 0; n < NT; ++n) *(LAS f32x4*)(rdp + n * 1024) = oc[n];
      asm volatile("" ::: "memory");
      *pair = 1u;
      if constexpr (ssm) {
        if (!fgot) {
          wait_tag<2>(ws_rs, WS_FA + fg * 1024 + lane * 8, tag); wait_tag<2>(ws_rs, WS_FA + fg * 1024 + 512 + lane * 8, tag);
          fetch_rows(true, lane);
        }
        stage_rows();
        asm volatile("" ::: "memory");
#pragma unroll
        for (int i = 0; i < 5; ++i) {
          const unsigned c = i * 64 + lane, r = (c * 7282u) >> 16;
          __builtin_amdgcn_raw_buffer_store_b128(lds16(lds + LA_FIMG + w * 4608 + c * 16), out_rs, c < 288u ? (fr * 128 + w * 32) * 2304 + 144 * fg + r * 2160u + c * 16u : 2048u * 2304u, 0, OUTA_AUX);
        }
      }
    } else {
      f32x4 pr[NT];
#pragma unroll 1
      for (unsigned sp = 0; sp < SPIN_CAP * 16; ++sp) {
        const unsigned go = *pair;
        asm volatile("" ::: "memory");
#pragma unroll
        for (int n = 0; n < NT; ++n) pr[n] = *(LAS f32x4*)(rdp + n * 1024);
        if (__builtin_amdgcn_readfirstlane(go)) break;
        asm volatile("" ::: "memory");
      }
#pragma unroll
      for (int n = 0; n < NT; ++n) *(LAS v2u*)(stw + n * 32) = pack_4(oc[n] + pr[n]);
      asm volatile("" ::: "memory");
      v4u v[NS];
#pragma unroll
      for (int j = 0; j < NS; ++j) v[j] = lds16(str[j]);
#pragma unroll
      for (int j = 0; j < NS; ++j) __builtin_amdgcn_raw_buffer_store_b128(v[j], out_rs, sto[j], 0, ssm ? OUTC_AUX : OUTB_AUX);
    }
    if (!ssm && blk == 0 && tid == 256) flag_store(g_ws + WS_EPOCH, tag);
    if (ssm) WAITCNT(0, 15);
  };

  if (role < 2) {
    if (role == 0) {
      __builtin_amdgcn_s_setprio(3);
      x_off0 += 128; x_off1 += 128;
      const char* wbt = g_w;
      auto issue_w = [&](auto T) {
        constexpr int o = wslot(T);
        wbt += 64 * 3456; asm volatile("" : "+s"(wbt));
        glds<o>(wbt, w_off[0], l1k); glds<o + 4096>(wbt, w_off[1], l1k); glds<o + 8192>(wbt, w_off[2], l1k); if (lw < 3) glds<o + 12288>(wbt, w_off[3], l1k);
      };
      auto issue_xa = [&](auto T) {
        constexpr int o = xslot(T);
        glds<o>(xb0, x_off0, l8k); glds<o + 1024>(xb0, x_off1, l8k); glds<o + 2048>(xb1, x_off0, l8k); glds<o + 3072>(xb1, x_off1, l8k);
      };
      auto issue_xb = [&](auto T) {
        constexpr int o = xslot(T);
        glds<o + 4096>(xb2, x_off0, l8k); glds<o + 5120>(xb2, x_off1, l8k); glds<o + 6144>(xb3, x_off0, l8k); glds<o + 7168>(xb3, x_off1, l8k);
        x_off0 += 128; x_off1 += 128; asm volatile("" : "+v"(x_off0), "+v"(x_off1));
      };
      issue_w(IC(1));
      asm volatile("s_waitcnt vmcnt(3)" :: "v"(cpf) : "memory"); BAR(); asm volatile("" ::: "memory");
      v4u tb[6];
#pragma unroll
      for (int i = 0; i < 6; ++i) tb[i] = lds16(lds + xslot(2) + lw * 8192 + i * 1024 + lane * 16);
      const unsigned wof[5] = {tb[0].x, tb[0].y, tb[0].z, tb[0].w, tb[1].x}, oe[5] = {tb[1].y, tb[1].z, tb[1].w, tb[2].x, tb[2].y};
      const unsigned o0 = tb[2].z, o1 = tb[2].w, o2 = tb[3].x, po = tb[3].y, pol = tb[3].z, po2 = tb[3].w, fto = tb[5].y, fho = tb[5].z;
      const lptr pa = (lptr)tb[4].x, pb = (lptr)tb[4].y, pe = (lptr)tb[4].z, d1 = (lptr)tb[4].w, d2 = (lptr)tb[5].x;
      auto wo_piece = [&](auto I) { glds<L_WO + I * 4096 - 4096>(wob + (I / 5) * 128 * 1152, wof[I % 5], lw1k); };
      issue_xa(IC(1));
      WAITCNT(63, 0);
      static_for<9>([&](auto T) {
        constexpr int t = T;
        if constexpr (t < 8) issue_xb(IC(t + 1));
        if constexpr (t < 7) { issue_w(IC(t + 2)); issue_xa(IC(t + 2)); }
        if constexpr (t == 7) static_for<WO7>([&](auto I) { wo_piece(I); });
        if constexpr (t == 8) static_for<WO8>([&](auto I) { wo_piece(IC(WO7 + I)); });
        if constexpr (t < 7) asm volatile("s_waitcnt vmcnt(7)" ::: "memory");
        if constexpr (t == 7) { asm volatile("" ::: "memory"); WAITCNT(WO7, 15); }
        BAR();
      });
      __builtin_amdgcn_s_setprio(LPRIO);
      v2u ftv;
      asm volatile("global_load_dwordx2 %0, %1, %2 sc1" : "=v"(ftv) : "v"(fto), "s"(g_ws) : "memory");
      static_for<23 - WO7 - WO8>([&](auto I) { if (WO7 + WO8 + I < 22 || lw < 2) wo_piece(IC(WO7 + WO8 + I)); });
      tag = read_tag(g_ws);
      asm volatile("s_waitcnt vmcnt(%1)" : "+v"(ftv) : "i"(22 - WO7 - WO8) : "memory");
      if (w == 7) {
        if (!__all(ftv.x == (unsigned)tag && ftv.y == (unsigned)(tag >> 32))) wait_tag(ws_rs, fto, tag);
        *(volatile LAS unsigned*)(lds + LB_KVF + KV_GO + 16) = 2;
      }
      STAMP(1);
      {
        f32x4 c0 = {0.f, 0.f, 0.f, 0.f}, c1 = c0, c2 = c0;
#pragma unroll
        for (int hf = 0; hf < 2; ++hf) {
          if (hf == 1) {
      {
        const v4u em = {EMPTY, EMPTY, EMPTY, EMPTY};
#pragma unroll
        for (int i = 0; i < 5; ++i) __builtin_amdgcn_raw_buffer_store_b128(em, ws_rs, oe[i], 0, 16);
      }
            __builtin_amdgcn_s_setprio(3);
          }
#pragma unroll 1
          for (unsigned spins = 0; spins < SPIN_CAP; ++spins) {
            if (__all(*(volatile LAS unsigned*)(lds + LB_KVF + KV_GO + min(lane, 3 + hf) * 4) > (unsigned)hf)) break;
            if (hf == 0) __builtin_amdgcn_s_sleep(1); else asm volatile("s_nop 7");
          }
          asm volatile("" ::: "memory");
          if (hf == 1 && tid == 448) flag_store(g_ws + WS_FH + hp * 128 + rb * 8, tag);
          if (hf == 1) STAMP(3);
          v4u A[4], B[4], E[4];
#pragma unroll
          for (int ks = 0; ks < 4; ++ks) {
            const int o = (ks * 64 + hf * 32) * 224;
            A[ks] = trd2(pa + o, pa + o + 16 * 224); B[ks] = trd2(pb + o, pb + o + 16 * 224); E[ks] = trd2(pe + o, pe + o + 16 * 224);
          }
          SCHED();
          if (lw & 1) {
#pragma unroll
            for (int ks = 0; ks < 4; ++ks) { c0 = mfma_bf(A[ks], B[ks], c0); c1 = mfma_bf(A[ks], E[ks], c1); c2 = mfma_bf(E[ks], E[ks], c2); }
          } else {
#pragma unroll
            for (int ks = 0; ks < 4; ++ks) { c0 = mfma_bf(A[ks], B[ks], c0); c1 = mfma_bf(A[ks], E[ks], c1); c2 = mfma_bf(E[ks], B[ks], c2); }
          }
        }
        __builtin_amdgcn_raw_buffer_store_b128(nz4(__builtin_bit_cast(v4u, c0)), ws_rs, o0, 0, 16);
        __builtin_amdgcn_raw_buffer_store_b128(nz4(__builtin_bit_cast(v4u, c1)), ws_rs, o1, 0, 16);
        __builtin_amdgcn_raw_buffer_store_b128(nz4(__builtin_bit_cast(v4u, c2)), ws_rs, o2, 0, 16);
      }
      STAMP(4);
      f32x4 sa, sb;
#define F4(v) __builtin_bit_cast(f32x4, v)
      if (FH_ON) { if (wait_tag<1>(ws_rs, fho, tag)) __builtin_amdgcn_s_sleep(HOP1_SLEEP); } else __builtin_amdgcn_s_sleep(10);
      STAMP(2);
      {
        v4u pv[8], qv[4];
#pragma unroll
        for (int r = 0; r < 4; ++r) { qv[r] = __builtin_amdgcn_raw_buffer_load_b128(ws_rs, po2 + r * 1024, 0, 16); SCHED(); }
#pragma unroll
        for (int r = 0; r < 8; ++r) { pv[r] = __builtin_amdgcn_raw_buffer_load_b128(ws_rs, (r < 4 ? pol : po) + (r & 3) * 1024, 0, 16); SCHED(); }
        sb = ((F4(qv[0]) + F4(qv[1])) + F4(qv[2])) + F4(qv[3]);
        SCHED();
        sa = F4(pv[0]);
#pragma unroll
        for (int r = 1; r < 8; ++r) sa += F4(pv[r]);
      }
      STAMP(6);
      const f32x4 ck = sa + sb; const float c = (ck[0] + ck[1]) + (ck[2] + ck[3]);
      if (!__all(c == c)) {
        v4u p0, p1, p2, p3, p4, p5, p6, p7, q0, q1, q2, q3;
        asm volatile("" : "=v"(p0), "=v"(p1), "=v"(p2), "=v"(p3), "=v"(p4), "=v"(p5), "=v"(p6), "=v"(p7), "=v"(q0), "=v"(q1), "=v"(q2), "=v"(q3));
        unsigned have = 0;
#pragma unroll 1
        for (unsigned spins = 0; spins < SPIN_CAP; ++spins) {
          load12(p0, p1, p2, p3, p4, p5, p6, p7, q0, q1, q2, q3, po, po2, g_ws, have);
#define OKB(v, b) if (!(have >> (b) & 1u)) { if (__all(umax4(v, 0u) != EMPTY)) have |= 1u << (b); }
          OKB(p0, 0) OKB(p1, 1) OKB(p2, 2) OKB(p3, 3) OKB(p4, 4) OKB(p5, 5) OKB(p6, 6) OKB(p7, 7) OKB(q0, 8) OKB(q1, 9) OKB(q2, 10) OKB(q3, 11)
#undef OKB
          have = __builtin_amdgcn_readfirstlane(have);
          if (have == 0xfffu) break;
        }
        sa = ((((((F4(p0) + F4(p1)) + F4(p2)) + F4(p3)) + F4(p4)) + F4(p5)) + F4(p6)) + F4(p7);
        sb = ((F4(q0) + F4(q1)) + F4(q2)) + F4(q3);
      }
      DRAIN();
#undef F4
      *(LAS v2u*)d1 = pack_4(sa);
      { float a0 = sb[0], a1 = sb[1], a2 = sb[2], a3 = sb[3];
        swap32(a0, a2); swap32(a1, a3);
        *(LAS unsigned*)d2 = pk_bf16(a0 + a2, a1 + a3); }
      STAMP(5);
      lds_barrier();
      if (tid == 448) flag_store(g_ws + WS_FO + (rb * 16 + hp) * 8, tag);
      __builtin_amdgcn_s_setprio(0);
    } else {
      {
        const int h = lw >> 1;
        const unsigned lb = (unsigned)(size_t)lds;
        unsigned wof[5], oe[5];
#pragma unroll
        for (int i = 0; i < 5; ++i) {
          const unsigned c = (i * 4 + lw) * 64 + lane, r = (c * 6554u) >> 16;
          wof[i] = r * 992u + c * 16u;
        }
        const unsigned pa = lb + LB_IMG + (4 * g + tq) * 224 + lw * 32 + ((tp ^ ((g & 1) << 1)) << 3), pe = lb + LB_IMG + (4 * g + tq) * 224 + 128 + ((tp ^ g) << 3);
#pragma unroll
        for (int i = 0; i < 4; ++i) oe[i] = WS_OM + ((rb * 16 + 4 * lw + i) * 16 + hq) * 1024 + lane * 16;
        oe[4] = lane < 32 ? WS_OS + (rb * 16 + hq) * 2048 + lw * 512 + lane * 16 : WS_USED;
        const unsigned pbase = WS_PART + hp * 49152 + rb * 1024;
        const int rec = s16 == 12 ? 0 : s16 - 7 - 2 * h, rx = s16 == 12 ? 0 : s16 - 7 - 2 * g;
        const unsigned o1 = (s16 == 12 || rec == 1 || rec == 2) ? pbase + 4 * 8192 + ((lw * 4 + rec) * 4 + g) * 16 : WS_USED;
        const bool on = (lw & 1) ? (lw == 1 && g < 2 && (s16 == 12 || rx == 1 || rx == 2)) : g == h;
        const unsigned o2 = on ? pbase + 5 * 8192 + ((lw & 1) ? 32 + g * 3 + rx : h * 16 + s16) * 16 : WS_USED;
        const int l5 = lane & 31, l2 = lw < 2 ? lw * 32 + (tq == 3 ? l5 - 4 : l5) : (lw == 2 ? l5 : 32 + min(l5, 5));
        const unsigned pol = WS_PART + hq * 49152 + lw * 8192 + lane * 16, po2 = WS_PART + hq * 49152 + (4 + h) * 8192 + (lane >> 5) * 4096 + l2 * 16;
        int dst2;
        if (lw < 2) { const int a = l2 >> 4, r3 = (l2 >> 2) & 3, hh = a >> 1, gq = l2 & 3; dst2 = KV_EM + hh * 512 + (r3 ? 1 + 2 * hh + r3 : hh) * 64 + ((gq & 2) + (a & 1)) * 16 + (gq & 1) * 8; }
        else if (lw == 2) dst2 = KV_NX + l2 * 16 + (l2 >> 4) * 8;
        else { const int i = l2 - 32, hh = i >= 3, r3 = i - 3 * hh; dst2 = KV_EX + (r3 ? 1 + 2 * hh + r3 : hh) * 16 + hh * 8; }
        const unsigned d1 = lb + LB_KVF + h * 1024 + (((g & 2) + (lw & 1)) * 16 + s16) * 16 + (g & 1) * 8, d2 = lb + LB_KVF + dst2 + (lane >> 5) * 4;
        const unsigned fto = lw == 3 ? WS_FT + ((lane >> 3) * 16 + (hp ^ ((lane & 4) << 1))) * 32 + tp * 8 : 0u;
        const lptr tp_ = lds + xslot(2) + lw * 8192 + lane * 16;
        *(LAS v4u*)(tp_) = (v4u){wof[0], wof[1], wof[2], wof[3]}; *(LAS v4u*)(tp_ + 1024) = (v4u){wof[4], oe[0], oe[1], oe[2]};
        *(LAS v4u*)(tp_ + 2048) = (v4u){oe[3], oe[4], pbase + lw * 8192 + lane * 16, o1}; *(LAS v4u*)(tp_ + 3072) = (v4u){o2, pol + 4096, pol, po2};
        *(LAS v4u*)(tp_ + 4096) = (v4u){pa, pe + 32 + 32 * h, pe, d1}; *(LAS v4u*)(tp_ + 5120) = (v4u){d2, fto, (unsigned)(WS_FH + hq * 128 + (lane & 7) * 8), 0u};
      }
      lptr wa[2][3];
      wa[0][0] = lds + wslot(0) + (8 * g + 2 * tq) * 80 + ((72 * hp) & 15) + tp * 8;
      wa[0][1] = lds + wslot(0) + (8 * g + 2 * tq) * 80 + ((72 * (tp ? hp : hq)) & 15) + 32 + (tp < 2 ? tp : 2) * 5120;
      wa[0][2] = lds + wslot(0) + (8 * g + 2 * tq) * 80 + ((72 * hq) & 15) + tp * 8;
      wa[1][0] = wa[0][0] + xslot(2); wa[1][1] = wa[0][1] + xslot(2); wa[1][2] = wa[0][2] + xslot(2);
      lptr xa[2][2];
      xa[0][0] = lds + w * 8192 + s16 * 128 + ((g ^ (s16 >> 1)) << 4); xa[0][1] = lds + w * 8192 + s16 * 128 + (((4 + g) ^ (s16 >> 1)) << 4);
      xa[1][0] = xa[0][0] + xslot(2); xa[1][1] = xa[0][1] + xslot(2);
      asm volatile("" : "+v"(wa[0][0]), "+v"(wa[0][1]), "+v"(wa[0][2]), "+v"(wa[1][0]), "+v"(wa[1][1]), "+v"(wa[1][2]), "+v"(xa[0][0]), "+v"(xa[0][1]), "+v"(xa[1][0]), "+v"(xa[1][1]));
      v4u Am[3], As[3];
      auto read_f = [&](v2u (&wf)[14], v4u (&xf)[4], int t, int ks, int r) {
        if (r < 4) xf[r] = lds16(xa[t % 3 == 2][ks] + (t % 3 == 2 ? 0 : xslot(t)) + r * 2048);
        else if (r < 18) { const int n = (r - 4) >> 1; wf[r - 4] = trd(wa[t % 3 == 2][n == 6 ? 1 : (n < 2 ? 2 : 0)] + (t % 3 == 2 ? 0 : xslot(t)) + ks * 2560 + (n < 6 ? (n >> 1) * 5120 + (n & 1) * 40 : 0) + (r & 1) * 80); }
      };
      lptr gm, gs, gw; unsigned ge[12];
      { const int hh = s16 >> 3, e0 = hh * 648 + min(9 * (tq & 1) + 4 * w + tp, 17) * 2;
        gm = lds + LB_WPHI + e0 + (4 * g + 2 * hh) * 36; gs = lds + LB_WPHI + e0 + (hh ? 0 : 16 * 36);
        gw = lds + (w < 3 ? LB_WF + w * 3072 : LB_WF - 2048) + lane * 16; pin(gm); pin(gs); pin(gw); }
      const int gq = w < 3 ? 2048 : 0;
      auto wphi_step = [&](auto K) {
        constexpr int k = K;
        const bool hh = s16 >= 8;
        if constexpr (k < 8) ge[k] = *(LAS u16*)(gm + (k >> 2) * 2048 + (k & 3) * 36);
        else if constexpr (k < 12) ge[k] = *(LAS u16*)(gs + ((k >> 1) & 1) * 2048 + (k & 1) * 36);
        else if constexpr (k < 18) { ge[2 * (k - 12)] |= ge[2 * (k - 12) + 1] << 16; pin(ge[2 * (k - 12)]); }
        else if constexpr (k < 20) { constexpr int p = k - 18; *(LAS v4u*)(gw + (p ? gq : 0)) = (v4u){hh ? 0u : ge[4 * p], hh ? 0u : ge[4 * p + 2], hh ? ge[4 * p] : 0u, hh ? ge[4 * p + 2] : 0u}; }
        else *(LAS v4u*)(gw + 1024) = (v4u){(g == 1 && !hh) ? ge[8] : 0u, (g == 1 && hh) ? ge[8] : 0u, (g == 0 && !hh) ? ge[10] : 0u, (g == 0 && hh) ? ge[10] : 0u};
      };
      f32x4 acc[7][4];
      v2u wf0[14], wf1[14]; v4u xf0[4], xf1[4];
      float MN[2][2];
      lptr wfp = lds + LB_WF + lane * 16; asm volatile("" : "+v"(wfp));
      asm volatile("s_waitcnt vmcnt(0) lgkmcnt(0)" :: "v"(cpf) : "memory"); BAR(); asm volatile("" ::: "memory");
      STAMP(1);
#pragma unroll
      for (int r = 0; r < 18; ++r) read_f(wf0, xf0, 0, 0, r);
#define WF(f, n) (v4u){f[2 * (n)].x, f[2 * (n)].y, f[2 * (n) + 1].x, f[2 * (n) + 1].y}
      static_for<8>([&](auto T) {
        constexpr int t = T;
        static_for<28>([&](auto I) {
          constexpr int n = I >> 2, m = I & 3;
          SCHED();
          if constexpr (t == 0) mfma_new(acc[n][m], WF(wf0, n), xf0[m]); else mfma_acc(acc[n][m], WF(wf0, n), xf0[m]);
          SCHED();
          if constexpr (I >= 4) read_f(wf1, xf1, t, 1, I - 4);
          if constexpr (t == 1 && I >= 22) { wphi_step(IC(2 * (I - 22))); wphi_step(IC(2 * (I - 22) + 1)); }
          if constexpr (t == 3 && I == 24) *(LAS v4u*)(wfp + (LB_KVF - LB_WF) + w * 1024) = (v4u){0u, 0u, 0u, 0u};
        });
        static_for<28>([&](auto I) {
          constexpr int n = I >> 2, m = I & 3;
          SCHED();
          if constexpr (I == 10) { asm volatile("" ::: "memory"); WAITCNT(63, 0); BAR(); asm volatile("" ::: "memory"); SCHED(); }
          mfma_acc(acc[n][m], WF(wf1, n), xf1[m]);
          SCHED();
          if constexpr (t == 1 && I >= 1 && I < 10) wphi_step(IC(11 + I));
          if constexpr (I >= 10) read_f(wf0, xf0, t + 1, 0, t < 7 ? I - 10 : (I < 14 ? I - 10 : (I < 24 ? I - 6 : I - 20)));
        });
      });
      static_for<20>([&](auto I) {
        constexpr int n = 2 + (I >> 2), m = I & 3;
        constexpr int ord[18] = {0, 8, 9, 10, 11, 16, 17, 1, 2, 3, 12, 13, 14, 15, 4, 5, 6, 7};
        SCHED();
        mfma_acc(acc[n][m], WF(wf0, n), xf0[m]);
        SCHED();
        if constexpr (I < 18) read_f(wf1, xf1, 8, 1, ord[I]);
        if constexpr (I >= 14) { if constexpr (I & 1) As[(I - 14) >> 1] = lds16(wfp + ((I - 14) >> 1) * 3072 + 1024); else Am[(I - 14) >> 1] = lds16(wfp + ((I - 14) >> 1) * 3072); }
      });
      v2u Sp[4]; v4u Bm[4], Bs[4];
      f32x4 F[4][3];
      lptr rt, rt2, rp, rx;
      static_for<20>([&](auto I) {
        SCHED();
        if constexpr (I == 6) { asm volatile("" ::: "memory"); WAITCNT(63, 0); BAR(); asm volatile("" ::: "memory"); SCHED(); }
        if constexpr (I < 12) { constexpr int m = I / 3, n = I % 3 == 2 ? 6 : 2 + I % 3; acc[n][m] = mfma_bf(WF(wf1, n), xf1[m], acc[n][m]); }
        else { constexpr int m = (I - 12) >> 1, n = 4 + (I & 1); acc[n][m] = mfma_bf(WF(wf1, n), xf1[m], acc[n][m]); }
        SCHED();
        if constexpr (I == 1) {
          int ln = lane; asm volatile("" : "+v"(ln));
          const int g = ln >> 4, tq = (ln >> 2) & 3;
          const lptr row0 = lds + LB_IMG + (64 * w + (ln & 15)) * 224;
          rt = row0 + g * 32 + ((tq & 1) << 4); rt2 = row0 + g * 32 + (((tq & 1) ^ 1) << 4); rp = row0 + ((g ^ tq) << 3);
          rx = row0 + 128 + (((g >> 1) ^ tq) << 3) + (g & 1) * 4;
          pin(rt); pin(rt2); pin(rp); pin(rx);
        }
        if constexpr (I >= 8) {
          constexpr int m = (I - 8) / 3, k = (I - 8) % 3;
          if constexpr (k == 0) { const v2u b = pack_4(acc[2][m]); Bm[m].x = b.x; Bm[m].y = b.y; }
          if constexpr (k == 1) { const v2u b = pack_4(acc[3][m]); Bm[m].z = b.x; Bm[m].w = b.y; pin(Bm[m]); }
          if constexpr (k == 2) { Sp[m] = pack_4(acc[6][m]); Bs[m] = (v4u){Sp[m].x, Sp[m].y, 0u, 0u}; pin(Bs[m]); Sp[m] = (v2u){Bs[m].x, Bs[m].y}; }
        }
      });
      STAMP(2);
      static_for<24>([&](auto I) {
        constexpr int m = I / 6, i = I % 3;
        SCHED();
        if constexpr (I % 6 < 3) F[m][i] = mfma_bf(Am[i], Bm[m], (f32x4){0.f, 0.f, 0.f, 0.f}); else F[m][i] = mfma_bf(As[i], Bs[m], F[m][i]);
        SCHED();
        if constexpr (I < 4) *(LAS v2u*)(rp + I * 16 * 224 + 128) = (v2u){g == 3 ? 0x3f803f80u : Sp[I].x, g == 3 ? 0x3f803f80u : Sp[I].y};
        else if constexpr (I < 12) *(LAS v2u*)(rp + ((I - 4) >> 1) * 16 * 224 + 160 + (I & 1) * 32) = pack_4(acc[4 + (I & 1)][(I - 4) >> 1]);
        else if constexpr (I >= 14 && I < 18) row_range(F[(I - 14) >> 1], I & 1, MN[(I - 14) >> 1]);
      });
      SCHED();
      STAMP(4);
#pragma unroll
      for (int hf = 0; hf < 2; ++hf) {
        unsigned P[2][9];
        if (hf == 1) { row_range(F[2], 0, MN[0]); row_range(F[2], 1, MN[0]); row_range(F[3], 0, MN[1]); row_range(F[3], 1, MN[1]); }
        features2(F[2 * hf], F[2 * hf + 1], MN, P[0], P[1]);
        asm volatile("" ::: "memory");
#pragma unroll
        for (int k = 0; k < 2; ++k) {
          const int o = (2 * hf + k) * 16 * 224;
          *(LAS v4u*)(rt + o) = (v4u){P[k][0], P[k][1], P[k][2], P[k][3]}; *(LAS v4u*)(rt2 + o) = (v4u){P[k][4], P[k][5], P[k][6], P[k][7]};
          *(LAS unsigned*)(rx + o) = P[k][8];
        }
        asm volatile("" ::: "memory");
        *(volatile LAS unsigned*)(lds + LB_KVF + KV_GO + w * 4) = hf + 1;
        if (hf == 0) STAMP(6);
        SCHED();
      }
      STAMP(3);
      unsigned Q[4][9];
#pragma unroll
      for (int i = 0; i < 3; ++i) Am[i] = lds16(wfp + i * 3072 + 2048);
      SCHED();
      static_for<16>([&](auto I) {
        constexpr int m = I >> 2, n = I & 1;
        if constexpr (I & 2) acc[n][m] = mfma_bf(WF(wf1, n), xf1[m], acc[n][m]); else acc[n][m] = mfma_bf(WF(wf0, n), xf0[m], acc[n][m]);
      });
#pragma unroll
      for (int m = 0; m < 4; ++m) {
        const v2u b0 = pack_4(acc[0][m]), b1 = pack_4(acc[1][m]);
        const v4u bm = {b0.x, b0.y, b1.x, b1.y}, bs = {0u, 0u, Sp[m].x, Sp[m].y};
#pragma unroll
        for (int i = 0; i < 3; ++i) F[m][i] = mfma_bf(As[i], bs, mfma_bf(Am[i], bm, (f32x4){0.f, 0.f, 0.f, 0.f}));
      }
#pragma unroll
      for (int hf = 0; hf < 2; ++hf) {
        row_range(F[2 * hf], 0, MN[0]); row_range(F[2 * hf], 1, MN[0]); row_range(F[2 * hf + 1], 0, MN[1]); row_range(F[2 * hf + 1], 1, MN[1]);
        if (hf == 0) SCHED();
        features2(F[2 * hf], F[2 * hf + 1], MN, Q[2 * hf], Q[2 * hf + 1]);
      }
      v4u B0[4], B1[4], Bx[4];
#pragma unroll
      for (int m = 0; m < 4; ++m) {
#pragma unroll
        for (int j = 0; j < 4; ++j) { auto r = __builtin_amdgcn_permlane32_swap(Q[m][j], Q[m][4 + j], false, false); B0[m][j] = r[0]; B1[m][j] = r[1]; }
        Bx[m] = (v4u){Q[m][8], 0u, 0u, 0u};
        asm volatile("" : "+v"(B0[m]), "+v"(B1[m]), "+v"(Bx[m]));
      }
      const int rec = tp < 2 ? tp : 2 * (tq & 1) + tp;
      lptr kn = lds + LB_KVF + lane * 16, ke = lds + LB_KVF + KV_EM + rec * 64 + g * 16, kx = lds + LB_KVF + KV_NX + s16 * 16 + g * 4, ky = lds + LB_KVF + KV_EX + rec * 16 + g * 4;
      unsigned om[4], os = WS_OS + ((rb * 16 + hq) * 256 + 64 * w + (g >> 1) * 16 + s16) * 8 + (g & 1) * 4;
#pragma unroll
      for (int m = 0; m < 4; ++m) om[m] = WS_OM + ((rb * 16 + 4 * w + m) * 16 + hq) * 1024 + lane * 16;
      v4u Nx0 = {0u, 0u, 0u, 0u}, Nx1 = Nx0, Ex = Nx0;
      asm volatile("" : "+v"(kn), "+v"(ke), "+v"(kx), "+v"(ky), "+v"(om[0]), "+v"(om[1]), "+v"(om[2]), "+v"(om[3]), "+v"(os), "+v"(Nx0), "+v"(Nx1), "+v"(Ex));
      STAMP(5);
      lds_barrier();
      STAMP(7);
      {
        const v4u Em0 = lds16(ke), Em1 = lds16(ke + 512);
        Ex.x = *(LAS unsigned*)ky;
        const v4u Nm0 = lds16(kn), Nm1 = lds16(kn + 1024);
        Nx0.x = *(LAS unsigned*)kx; Nx1.x = *(LAS unsigned*)(kx + 256);
        SCHED();
        const f32x4 z = {0.f, 0.f, 0.f, 0.f};
        f32x4 E[4], O0[4], O1[4];
#pragma unroll
        for (int m = 0; m < 4; ++m) E[m] = mfma_bf(Em0, B0[m], z);
#pragma unroll
        for (int m = 0; m < 4; ++m) E[m] = mfma_bf(Em1, B1[m], E[m]);
#pragma unroll
        for (int m = 0; m < 4; ++m) E[m] = mfma_bf(Ex, Bx[m], E[m]);
        SCHED();
        float r0[4], r1[4];
#pragma unroll
        for (int m = 0; m < 4; ++m) {
          O0[m] = mfma_bf(Nm0, B0[m], z); O1[m] = mfma_bf(Nm1, B1[m], z);
          O0[m] = mfma_bf(Nx0, Bx[m], O0[m]); O1[m] = mfma_bf(Nx1, Bx[m], O1[m]);
          r0[m] = __builtin_amdgcn_rcpf(E[m][0] + 1e-6f); r1[m] = __builtin_amdgcn_rcpf(E[m][1] + 1e-6f);
        }
        SCHED();
#pragma unroll
        for (int m = 0; m < 4; ++m) {
          const v2u o0 = pack_4(O0[m] * r0[m]), o1 = pack_4(O1[m] * r1[m]);
          __builtin_amdgcn_raw_buffer_store_b128(nz4((v4u){o0.x, o0.y, o1.x, o1.y}), ws_rs, om[m], 0, 16);
          SCHED();
        }
#pragma unroll
        for (int m = 0; m < 4; m += 2) {
          const float ra = (g & 1) ? r1[m] : r0[m], rb2 = (g & 1) ? r1[m + 1] : r0[m + 1], rs = g < 2 ? ra : rb2;
          __builtin_amdgcn_raw_buffer_store_b32(nz(pk_bf16((g < 2 ? E[m][2] : E[m + 1][2]) * rs, (g < 2 ? E[m][3] : E[m + 1][3]) * rs)), ws_rs, os, m * 128, 16);
        }
      }
    }
    STAMP(8);
    project(IC(5));
  } else {
    const lptr img = lds + (w < 4 ? w : 4) * LA_CH;
    unsigned ka[8];
    auto set_ka = [&]() {
      const unsigned kb = (unsigned)(size_t)img + 16 + 2 * (2032 - 6 - 1792 - 16 * s16 + 4 * g);
#pragma unroll
      for (int k = 0; k < 4; ++k) { ka[k] = kb + ((k + tq) & 3) * (LA_KC + 4); ka[4 + k] = ka[k] + 4; }
    };
    unsigned prm[4], pdt, pds;
    f32x4 y[8];
    v4u wv[18];
    auto load_prm = [&]() {
#pragma unroll
      for (int i = 0; i < 4; ++i) prm[i] = *(LAS unsigned*)((lptr)lprm + i * 256 + lane * 4);
      asm volatile("" ::: "memory");
    };
    lptr kp = img + tp * LA_KC + 16 + 2 * (2040 - 1792 - 224 + 8 * (g - tq));
    lptr up = img + LA_U0 + (s16 & 1) * LA_UC + 2 * (32 * (s16 >> 1) + 8 * g);
    if (role == 3) {
      asm volatile("s_load_dword %0, %2, %4\n\ts_load_dword %1, %3, %4\n\ts_waitcnt lgkmcnt(0)" : "=&s"(pdt), "=&s"(pds) : "s"(g_ldt), "s"(g_dsk), "s"((ch >> 1) * 4) : "memory");
      pdt = (ch & 1) ? pdt & 0xffff0000u : pdt << 16; pds = (ch & 1) ? pds & 0xffff0000u : pds << 16;
      asm volatile("s_waitcnt vmcnt(0)" :: "v"(cpf) : "memory");
      load_prm();
      STAMP(6);
      const Modes M = prep_modes(prm, pdt);
      const lptr st = img + lane * 8, rd = img + g * 16;
      const cplx2 e1 = cexp2(M.al, M.wr, 1.f), e256 = cexp2(M.al, M.wr, 256.f), e2 = cmul(e1, e1), e4 = cmul(e2, e2), e8 = cmul(e4, e4);
      const cplx2 b1 = cmul(M.C, e256), b2 = cmul(b1, e256), b3 = cmul(b2, e256);
      v4u Y[8], A[8], B[8];
      v2u kd[8];
      if (J2_AT == 3) prefetch(IC(2));
      gen_y<544>(st, M);
      if (J2_AT == 2) prefetch(IC(2));
      { cplx2 b = b3;
#pragma unroll
        for (int t = 0; t < 4; ++t) { b = cmul(b, e256); gen_run<3, 544>(st + (16 + 3 * t) * 544, b, e8); } }
      asm volatile("" ::: "memory");
      read8(Y, rd + s16 * 544); read8(A, rd + (16 + row_t47(tq, tp)) * 544); read8(B, rd + (22 + row_t47(tq, tp)) * 544);
      asm volatile("" ::: "memory");
      gen_run<16, 544>(st, M.C, e1); gen_run<5, 544>(st + 16 * 544, b2, e4); gen_run<5, 544>(st + 21 * 544, b3, e4);
      { const f32x4 k0 = dot8(A, Y, M.kscale), k1 = dot8(B, Y, M.kscale);
        kd[4] = pack_s8(k0[0], k0[1]); kd[5] = pack_s8(k0[2], k0[3]); kd[6] = pack_s8(k1[0], k1[1]); kd[7] = pack_s8(k1[2], k1[3]); }
      asm volatile("" ::: "memory");
      read8(A, rd + (15 - s16) * 544); read8(B, rd + (16 + row_t23(tq, tp)) * 544);
      asm volatile("" ::: "memory");
      gen_run<9, 544>(st, b1, e2);
      { f32x4 k0 = dot8(A, Y, M.kscale); const f32x4 k1 = dot8(B, Y, M.kscale);
        if (lane == 48) k0[3] += __uint_as_float(pds);
        kd[0] = pack_4(k0); kd[2] = pack_s4(k1[0], k1[1], g & 1); kd[3] = pack_s4(k1[2], k1[3], g & 1); }
      asm volatile("" ::: "memory");
      read8(A, rd + row_t1(s16) * 544);
      asm volatile("" ::: "memory");
      STAMP(1);
      set_ka();
      put_k2<6>(ka, kd[6], kd[7]); put_k2<4>(ka, kd[4], kd[5]); put_k2<2>(ka, kd[2], kd[3]);
      kd[1] = pack_4(dot8(A, Y, M.kscale));
      put_k2<0>(ka, kd[0], kd[1]);
      if (s16 < 15 - g) *(LAS unsigned*)(img + g * LA_KC + 16 + 2 * (2042 + 2 * g) + s16 * 4) = 0u;
      STAMP(7);
#pragma unroll
      for (int t = 0; t < 8; ++t) { y[t] = (f32x4){0.f, 0.f, 0.f, 0.f}; asm volatile("" : "+v"(y[t])); }
      asm volatile("" : "+v"(kp), "+v"(up) :: "memory");
      v4u Kf[2][8], Uf[2][8];
#pragma unroll
      for (int d = 0; d < 8; ++d) Kf[0][d] = lds16(kp + 512 * (7 - d));
      lds_barrier();
      STAMP(2);
#pragma unroll
      for (int d = 0; d < 8; ++d) Uf[0][d] = lds16(up + 512 * d);
#pragma unroll 1
      for (int sp = 0; sp < CONV_IT; ++sp) {
        static_for<2>([&](auto CU) {
          constexpr int cu = CU, nx = cu ^ 1;
          static_for<8>([&](auto TT) {
            constexpr int t = TT;
            static_for<t + 1>([&](auto DD) {
              constexpr int d = DD, i = t * (t + 1) / 2 + d;
              SCHED();
              mfma_acc(y[t], Uf[cu][t - d], Kf[cu][d]);
              SCHED();
              if constexpr ((i & 1) == 1 && i < 32) {
                constexpr int f = i >> 2;
                if constexpr ((i & 2) == 0) Uf[nx][f] = lds16(up + 64 * (cu + 1) + 512 * f); else Kf[nx][f] = lds16(kp + 64 * (cu + 1) + 512 * (7 - f));
              }
            });
          });
        });
        kp += 128; up += 128;
      }
      MFMA_FENCE();
      const unsigned ob = (unsigned)(size_t)(lds + LA_OUT + w * 4128) + 4 * (32 * g + s16);
#pragma unroll
      for (int t = 0; t < 8; t += 2) {
        const v2u a = pack_4(y[t]), b = pack_4(y[t + 1]);
        asm volatile("ds_write2_b32 %0, %1, %2 offset0:0 offset1:16\n\tds_write2_b32 %0, %3, %4 offset0:128 offset1:144" :: "v"(ob + 512 * t), "v"(a.x), "v"(a.y), "v"(b.x), "v"(b.y) : "memory");
      }
    } else {
      const int h = w - 4, t2 = tid - 256;
      const lptr hs = lds + LA_HS + h * LA_HSW;
      __builtin_amdgcn_s_setprio(2);
      asm volatile("s_load_dword %0, %2, %4\n\ts_load_dword %1, %3, %4\n\ts_waitcnt lgkmcnt(0)" : "=&s"(pdt), "=&s"(pds) : "s"(g_ldt), "s"(g_dsk), "s"((ch >> 1) * 4) : "memory");
      pdt = (ch & 1) ? pdt & 0xffff0000u : pdt << 16; pds = (ch & 1) ? pds & 0xffff0000u : pds << 16;
      tag = read_tag(g_ws);
      asm volatile("s_waitcnt vmcnt(6)" :: "v"(cpf) : "memory");
      load_prm();
      v4u uv[5]; unsigned uh[5];
      {
        auto regroup = [&]() {
          DRAIN();
          STAMP_(6);
          const lptr tb = lds + h * LA_CH + LA_U0 + (8 * tp + tq) * 144 + g * 8;
          const unsigned uo = WS_UG + (18 * tg_ + g) * 16384 + (128 * tr + 32 * h) * 8 + s16 * 16;
          v4u tv[5];
#pragma unroll
          for (int i = 0; i < 5; ++i) tv[i] = trd2(tb + i * 32, tb + i * 32 + 4 * 144);
#pragma unroll
          for (int i = 0; i < 4; ++i) __builtin_amdgcn_raw_buffer_store_b128(tv[i], ws_rs, uo + i * 65536, 0, 16);
          __builtin_amdgcn_raw_buffer_store_b128(tv[4], ws_rs, g < 2 ? uo + 4 * 65536 : WS_USED, 0, 16);
          *(LAS u64*)(lds + LA_SYNC + h * 8) = tag;
          const v4u em = {EMPTY, EMPTY, EMPTY, EMPTY};
          const unsigned o3 = WS_PART + tr * 49152 + h * 8192 + tg_ * 1024 + lane * 16;
          __builtin_amdgcn_raw_buffer_store_b128(em, ws_rs, o3, 0, 16);
          if (h < 2) __builtin_amdgcn_raw_buffer_store_b128(em, ws_rs, o3 + 4 * 8192, 0, 16);
          SCHED();
        };
        auto announce = [&]() {
          SCHED();
          DRAIN();
          if (lane == 0) flag_store(g_ws + WS_FT + ((tg_ * 16 + tr) * 4 + h) * 8, tag);
          STAMP_(1);
          if (aj >= 6 && lw < 2) prefetch(IC(3));
          glds4<0>(g_wo, (aj * 256 + t2) * 128u, (unsigned)(size_t)lds + LDS_BYTES - 256);
          if (aj >= 11) glds4<0>(g_wo, min(4096u + (aj - 11) * 256 + t2, 5183u) * 128u, (unsigned)(size_t)lds + LDS_BYTES - 256);
        };
        Modes M = prep_modes(prm, pdt);
        const lptr st = hs + lane * 8, rd = hs + g * 16;
        v4u Y[8], A[8];
        if (T_AT == 2) { pin(M.al); pin(M.wr); pin(M.C.r); pin(M.C.i); regroup(); }
        cplx2 es = cexp2(M.al, M.wr, (float)(1 << h)), e256 = cexp2(M.al, M.wr, 256.f), b = cmul(M.C, cexp2(M.al, M.wr, (float)((128 << h) & ~128)));
        pin(es.r); pin(es.i); pin(e256.r); pin(e256.i); pin(b.r); pin(b.i);
        if (T_AT == 1) regroup();
        gen_y<528>(st, M);
        asm volatile("" ::: "memory");
        read8(Y, rd + s16 * 528);
        asm volatile("" ::: "memory");
        if (T_AT == 0) regroup();
        set_ka();
        announce();
        if (h == 0) gen_run<16, 528>(st, b, es);
        else if (h == 1) gen_run<9, 528>(st, b, es);
        else if (h == 2) { gen_run<5, 528>(st, b, es); gen_run<5, 528>(st + 5 * 528, cmul(b, e256), es); }
        else {
#pragma unroll
          for (int t = 0; t < 4; ++t) { gen_run<3, 528>(st + 3 * t * 528, b, es); if (t < 3) b = cmul(b, e256); }
        }
        asm volatile("" ::: "memory");
        const lptr ra = rd + (h == 0 ? 15 - s16 : h == 1 ? row_t1(s16) : h == 2 ? row_t23(tq, tp) : row_t47(tq, tp)) * 528;
        v4u B[8];
        read8(A, ra);
        if (h == 3) read8(B, ra + 6 * 528); else asm volatile("" : "=v"(B[0]), "=v"(B[1]), "=v"(B[2]), "=v"(B[3]), "=v"(B[4]), "=v"(B[5]), "=v"(B[6]), "=v"(B[7]));
        SCHED();
        f32x4 k0 = dot8(A, Y, M.kscale);
        if (h == 0) {
          if (lane == 48) k0[3] += __uint_as_float(pds);
          put_k1<0>(ka, pack_4(k0));
          if (s16 < 15 - g) *(LAS unsigned*)(img + g * LA_KC + 16 + 2 * (2042 + 2 * g) + s16 * 4) = 0u;
        } else if (h == 1) put_k1<1>(ka, pack_4(k0));
        else if (h == 2) put_k2<2>(ka, pack_s4(k0[0], k0[1], g & 1), pack_s4(k0[2], k0[3], g & 1));
        else {
          const f32x4 k1 = dot8(B, Y, M.kscale);
          put_k2<4>(ka, pack_s8(k0[0], k0[1]), pack_s8(k0[2], k0[3])); put_k2<6>(ka, pack_s8(k1[0], k1[1]), pack_s8(k1[2], k1[3]));
        }
      }
      STAMP(7);
      {
        __builtin_amdgcn_s_sleep(FT_PRE);
        wait_tag<FT_SLEEP>(ws_rs, WS_FT + xcd * 512 + max(h * 16 + min(lane, 16) - 1, 0) * 8, tag);
        STAMP(4);
        const unsigned mo = WS_UG + (18 * xcd + aj) * 16384 + h * 4096 + lane * 16;
        const unsigned xo = WS_UG + (18 * xcd + 16 + (aj >> 3)) * 16384 + t2 * 64 + ((aj >> 1) & 3) * 16;
#pragma unroll
        for (int i = 0; i < 4; ++i) uv[i] = __builtin_amdgcn_raw_buffer_load_b128(ws_rs, mo, i * 1024, 16);
        uv[4] = __builtin_amdgcn_raw_buffer_load_b128(ws_rs, xo, 0, 16);
        const unsigned hl = __builtin_amdgcn_raw_buffer_load_b32(ws_rs, h == 0 ? WS_USED : (lane < 4 ? mo : xo - lane * 64) - 52, 0, 16);
#pragma unroll 1
        for (unsigned spins = 0; spins < SPIN_CAP; ++spins) {
          if (__all(*(volatile LAS u64*)(lds + LA_SYNC + tp * 8) == tag)) break;
          __builtin_amdgcn_s_sleep(1);
        }
        asm volatile("" ::: "memory");
        { const int ci = t2 / 28, k = t2 - ci * 28;
          *(LAS v4u*)(lds + (ci >> 1) * LA_CH + LA_U0 + (ci & 1) * LA_UC + k * 16) = (v4u){0u, 0u, 0u, 0u};
          if (t2 < 24) *(LAS v4u*)(lds + 4 * LA_CH + LA_U0 + LA_UC + (4 + t2) * 16) = (v4u){0u, 0u, 0u, 0u}; }
        SCHED();
        { unsigned prev = hl;
#pragma unroll
          for (int i = 0; i < 4; ++i) { const unsigned r = __builtin_amdgcn_ds_bpermute(((lane - 4) & 63) * 4, uv[i].w); uh[i] = lane < 4 ? prev : r; prev = r; }
          uh[4] = __builtin_amdgcn_update_dpp(__builtin_amdgcn_readlane(hl, 4), uv[4].w, 0x138, 0xf, 0xf, false); }
#pragma unroll
        for (int i = 0; i < 5; ++i) {
          const lptr p = i < 4 ? lds + tp * LA_CH + LA_U0 + 448 + (64 * h + 16 * i + (lane >> 2)) * 16 : lds + 4 * LA_CH + LA_U0 + 448 + t2 * 16;
          const v4u v = uv[i];
          *(LAS v4u*)(p + LA_UC) = v;
          *(LAS v4u*)p = (v4u){__builtin_amdgcn_alignbit(v.x, uh[i], 16), __builtin_amdgcn_alignbit(v.y, v.x, 16), __builtin_amdgcn_alignbit(v.z, v.y, 16), __builtin_amdgcn_alignbit(v.w, v.z, 16)};
        }
      }
      STAMP(5);
      __builtin_amdgcn_s_setprio(0);
      lds_barrier();
      STAMP(2);
      __builtin_amdgcn_s_sleep(HC_PRE);
      {
        kp += 128 * h; up += 128 * h;
        v4u Kf[2][8], Uf[2][8];
#pragma unroll
        for (int s = 0; s < 2; ++s)
#pragma unroll
          for (int d = 0; d < 8; ++d) { Uf[s][d] = lds16(up + 64 * s + 512 * d); Kf[s][d] = lds16(kp + 64 * s + 512 * (7 - d)); }
#pragma unroll
        for (int i = 0; i < 4; ++i) y[i] = (f32x4){0.f, 0.f, 0.f, 0.f};
        WAITCNT(63, 0);
        __builtin_amdgcn_s_setprio(2);
        SCHED();
        auto part = [&](auto T0, auto T1, auto T2, auto T3) {
#pragma unroll
          for (int s = 0; s < 2; ++s) {
#pragma unroll
            for (int d = 0; d <= T3; ++d) y[3] = mfma_bf(Uf[s][T3 - d], Kf[s][d], y[3]);
#pragma unroll
            for (int d = 0; d <= T2; ++d) y[2] = mfma_bf(Uf[s][T2 - d], Kf[s][d], y[2]);
#pragma unroll
            for (int d = 0; d <= T1; ++d) y[1] = mfma_bf(Uf[s][T1 - d], Kf[s][d], y[1]);
#pragma unroll
            for (int d = 0; d <= T0; ++d) y[0] = mfma_bf(Uf[s][T0 - d], Kf[s][d], y[0]);
          }
        };
        if (aj & 1) part(IC(6), IC(5), IC(2), IC(1)); else part(IC(7), IC(4), IC(3), IC(0));
#pragma unroll
        for (int i = 0; i < 4; ++i) *(LAS f32x4*)(lds + LA_XP + (i * 4 + h) * 1024 + lane * 16) = y[i];
      }
      __builtin_amdgcn_s_setprio(0);
      __builtin_amdgcn_s_sleep(WV_PRE);
      SCHED();
#pragma unroll
      for (int i = 0; i < 18; ++i) wv[i] = *(const v4u*)(wob + i * 32 * 1152 + (unsigned)((t2 >> 3) * 1152 + (t2 & 7) * 16));
#pragma unroll
      for (int i = 0; i < 18; i += 3) asm volatile("" : "+v"(wv[i]), "+v"(wv[i + 1]), "+v"(wv[i + 2]));
    }
    lds_barrier();
    STAMP(3);
    if (role == 3) {
      const lptr ob = lds + LA_OUT + tid * 4;
#pragma unroll
      for (int i = 0; i < 4; ++i) {
        const unsigned a = *(LAS unsigned*)(ob + i * 1024), b = *(LAS unsigned*)(ob + i * 1024 + 4128), c = *(LAS unsigned*)(ob + i * 1024 + 2 * 4128), d = *(LAS unsigned*)(ob + i * 1024 + 3 * 4128);
        const v4u r = {__builtin_amdgcn_perm(b, a, 0x05040100u), __builtin_amdgcn_perm(d, c, 0x05040100u), __builtin_amdgcn_perm(b, a, 0x07060302u), __builtin_amdgcn_perm(d, c, 0x07060302u)};
        __builtin_amdgcn_raw_buffer_store_b128(r, ws_rs, WS_UG + (18 * xcd + aj) * 16384 + tid * 16 + i * 4096, 0, 16);
        if (AG_GAP && i < 3) __builtin_amdgcn_s_sleep(AG_GAP);
      }
      *(LAS unsigned*)(lds + LA_SYNC + 32 + w * 4) = 0u;
      const v2u tg = *(LAS v2u*)(lds + LA_SYNC);
      tag = (u64)(unsigned)__builtin_amdgcn_readfirstlane(tg.x) | ((u64)(unsigned)__builtin_amdgcn_readfirstlane(tg.y) << 32);
    } else {
      const lptr xp = lds + LA_XP + lw * 4096 + lane * 16;
      const f32x4 v = (*(LAS f32x4*)xp + *(LAS f32x4*)(xp + 1024)) + (*(LAS f32x4*)(xp + 2048) + *(LAS f32x4*)(xp + 3072));
      const int xt = ((aj & 1) ? 0x1256 : 0x0347) >> (4 * lw) & 7;
      const unsigned o = WS_AX + (xcd * 8 + (aj >> 1)) * 4096 + xt * 512 + g * 128 + s16 * 4;
      const v2u p = pack_4(v);
      __builtin_amdgcn_raw_buffer_store_b32(p.x, ws_rs, o, 0, 16); __builtin_amdgcn_raw_buffer_store_b32(p.y, ws_rs, o + 64, 0, 16);
      const lptr wla = lds + L_WO + ((tid - 256) >> 3) * 160 + (tid & 7) * 16;
#pragma unroll
      for (int i = 0; i < 18; ++i) *(LAS v4u*)(wla + i * 32 * 160) = wv[i];
    }
    lds_barrier();
    STAMP(8);
    project(IC(4));
  }
}

extern "C" void kernel_launch(void* const* d_in, const int* in_sizes, int n_in, void* d_out, int out_size, void* d_ws, size_t ws_size,
                              hipStream_t stream) {
  (void)in_sizes; (void)n_in; (void)out_size; (void)ws_size;
  (void)hipFuncSetAttribute((const void*)fused_kernel, hipFuncAttributeMaxDynamicSharedMemorySize, LDS_BYTES);
  fused_kernel<<<256, 512, LDS_BYTES, stream>>>((const char*)d_in[0], (const char*)d_in[1], (const char*)d_in[2], (const char*)d_in[3],
                                                (const char*)d_in[4], (const char*)d_in[5], (const char*)d_in[6], (const char*)d_in[7],
                                                (const char*)d_in[8], (const char*)d_in[9], (const char*)d_in[10], (char*)d_out, (char*)d_ws);
}
```
